```python
import torch

def _softmax(x, axis=-1):
    return torch.softmax(x, dim=axis)
def _gelu(x):
    return torch.nn.functional.gelu(x, approximate="tanh")
def _silu(x):
    return torch.nn.functional.silu(x)
def _sigmoid(x):
    return torch.sigmoid(x)
def _layernorm(x, axis=-1, eps=1e-5):
    m = x.mean(dim=axis, keepdim=True)
    v = x.var(dim=axis, keepdim=True, unbiased=False)
    return (x - m) / torch.sqrt(v + eps)
def _scatter_add(tgt, idx, src, axis=0):
    return tgt.clone().index_add_(axis, idx, src)
def _segment_sum(src, seg, n):
    out = torch.zeros((n,) + src.shape[1:], dtype=src.dtype)
    return out.index_add_(0, seg, src)
def _pad(x, pw):
    flat = tuple(v for pair in reversed(pw) for v in pair)
    return torch.nn.functional.pad(x, flat)

kernel_name = 'gated_mix__spectral_fnet__glu_gated_glu__prompt'

def setup_inputs():

    _w = (lambda a: (lambda t: t.to(torch.bfloat16) if t.is_floating_point() else t)(torch.as_tensor(a)))
    def _A_setup():
        B, S, D = 1, 2048, 448
        ns = torch.arange(S)[:, None] * torch.arange(S)[None, :]
        nd = torch.arange(D)[:, None] * torch.arange(D)[None, :]
        Ws_re = torch.cos(-2.0 * torch.pi * ns / S).to(float)
        Ws_im = torch.sin(-2.0 * torch.pi * ns / S).to(float)
        Wd_re = torch.cos(-2.0 * torch.pi * nd / D).to(float)
        Wd_im = torch.sin(-2.0 * torch.pi * nd / D).to(float)
        return {
            "x": _w((lambda *s: torch.randn(*s, dtype=torch.bfloat16) * 0.05)(B, S, D)),
            "dft_seq_re": _w(Ws_re),
            "dft_seq_im": _w(Ws_im),
            "dft_hid_re": _w(Wd_re),
            "dft_hid_im": _w(Wd_im),
        }
    _A_in = _A_setup()

    def _B_setup():
        B, S, D, F = 1, 2048, 448, 1792
        return {
            "x": _w((lambda *s: torch.randn(*s, dtype=torch.bfloat16) * 0.05)(B, S, D)),
            "w_gate": _w((lambda *s: torch.randn(*s, dtype=torch.bfloat16) * 1.6)(D, F)),
            "w_up": _w((lambda *s: torch.randn(*s, dtype=torch.bfloat16) * 0.8)(D, F)),
            "w_down": _w((lambda *s: torch.randn(*s, dtype=torch.bfloat16) * 0.8)(F, D)),
        }
    _B_in = _B_setup()

    return {
        "A_x": _A_in["x"],
        "A_dft_seq_re": _A_in["dft_seq_re"],
        "A_dft_seq_im": _A_in["dft_seq_im"],
        "A_dft_hid_re": _A_in["dft_hid_re"],
        "A_dft_hid_im": _A_in["dft_hid_im"],
        "B_x": _B_in["x"],
        "B_w_gate": _B_in["w_gate"],
        "B_w_up": _B_in["w_up"],
        "B_w_down": _B_in["w_down"],
        "gate_w": _w((lambda *s: torch.randn(*s, dtype=torch.bfloat16) * 0.05)(448) * 16.0),
    }

def reference(A_x, A_dft_seq_re, A_dft_seq_im, A_dft_hid_re, A_dft_hid_im, B_x, B_w_gate, B_w_up, B_w_down, gate_w):
    def _A_ref(x, dft_seq_re, dft_seq_im, dft_hid_re, dft_hid_im):
        a = torch.einsum("bsd,ed->bse", x, dft_hid_re)
        b = torch.einsum("bsd,ed->bse", x, dft_hid_im)
        y = torch.einsum("ts,bse->bte", dft_seq_re, a) - torch.einsum("ts,bse->bte", dft_seq_im, b)
        return y

    def _B_ref(x, w_gate, w_up, w_down):
        g = torch.einsum("bsd,df->bsf", x, w_gate)
        u = torch.einsum("bsd,df->bsf", x, w_up)
        h = _sigmoid(g) * u
        return torch.einsum("bsf,fd->bsd", h, w_down)

    x = A_x
    g = _sigmoid((x * gate_w).sum(-1, keepdims=True))
    a = _A_ref(x, A_dft_seq_re, A_dft_seq_im, A_dft_hid_re, A_dft_hid_im)
    b = _B_ref(x, B_w_gate, B_w_up, B_w_down)
    return g * a + (1 - g) * b
```

```cpp
#include <hip/hip_runtime.h>
#include <hip/hip_bf16.h>
typedef unsigned short u16;
constexpr int S=2048, D=448, F=1792;
__device__ inline float bf(u16 v){ return __uint_as_float(((unsigned)v)<<16); }
__device__ inline u16 tobf(float f){ __hip_bfloat16 b=__float2bfloat16(f); return *(u16*)&b; }

__global__ void k1(const u16* xa,const u16* cr,const u16* ci,const u16* xb,const u16* wg,const u16* wu,
                   float* P,float* Q,float* H){
  int idx=blockIdx.x*256+threadIdx.x;
  if(idx<S*D){
    int t=idx/D,d=idx%D; float p=0,q=0;
    for(int s=0;s<S;s++){ float x=bf(xa[s*D+d]); p+=bf(cr[t*S+s])*x; q+=bf(ci[t*S+s])*x; }
    P[idx]=bf(tobf(p)); Q[idx]=bf(tobf(q));
  } else {
    idx-=S*D;
    if(idx<S*F){
      int t=idx/F,f=idx%F; float g=0,u=0;
      for(int d=0;d<D;d++){ float x=bf(xb[t*D+d]); g+=x*bf(wg[d*F+f]); u+=x*bf(wu[d*F+f]); }
      H[idx]=bf(tobf(u/(1.f+__expf(-g))));
    }
  }
}
__global__ void k2(const u16* xa,const u16* wre,const u16* wim,const u16* wd,const u16* gw,
                   const float* P,const float* Q,const float* H,u16* out){
  int idx=blockIdx.x*256+threadIdx.x;
  if(idx>=S*D) return;
  int t=idx/D,e=idx%D; float a=0,b=0,z=0;
  for(int d=0;d<D;d++){ a+=P[t*D+d]*bf(wre[e*D+d])-Q[t*D+d]*bf(wim[e*D+d]); z+=bf(xa[t*D+d])*bf(gw[d]); }
  for(int f=0;f<F;f++) b+=H[t*F+f]*bf(wd[f*D+e]);
  float g=1.f/(1.f+__expf(-z));
  out[idx]=tobf(g*a+(1.f-g)*b);
}
extern "C" void kernel_launch(void* const* d_in, const int* in_sizes, int n_in,
                              void* d_out, int out_size, void* d_ws, size_t ws_size,
                              hipStream_t stream) {
  const u16* xa=(const u16*)d_in[0]; const u16* cr=(const u16*)d_in[1]; const u16* ci=(const u16*)d_in[2];
  const u16* wre=(const u16*)d_in[3]; const u16* wim=(const u16*)d_in[4]; const u16* xb=(const u16*)d_in[5];
  const u16* wg=(const u16*)d_in[6]; const u16* wu=(const u16*)d_in[7]; const u16* wd=(const u16*)d_in[8];
  const u16* gw=(const u16*)d_in[9];
  float* P=(float*)d_ws; float* Q=P+S*D; float* H=Q+S*D;
  int n1=S*D+S*F;
  k1<<<(n1+255)/256,256,0,stream>>>(xa,cr,ci,xa,wg,wu,P,Q,H);
  k2<<<(S*D+255)/256,256,0,stream>>>(xa,wre,wim,wd,gw,P,Q,H,(u16*)d_out);
}
```

```cpp
#include <hip/hip_runtime.h>
#include <stdint.h>

namespace {

typedef unsigned short u16;
typedef unsigned long long u64;
typedef __attribute__((ext_vector_type(8))) __bf16 bf16x8;
typedef __attribute__((ext_vector_type(4))) __bf16 bf16x4;
typedef __attribute__((ext_vector_type(2))) __bf16 bf16x2;
typedef __attribute__((ext_vector_type(4))) float f32x4;
typedef __attribute__((ext_vector_type(2))) float f32x2;
typedef __attribute__((ext_vector_type(4))) unsigned u32x4;
typedef __attribute__((ext_vector_type(2))) unsigned u32x2;
#define LDS3 __attribute__((address_space(3)))

constexpr int S = 2048, D = 448, F = 1792;
constexpr int NK2 = 84;
constexpr size_t HB_BYTES = (size_t)32 * NK2 * 4096;
constexpr size_t WB_OFF = HB_BYTES;
constexpr size_t WB_BYTES = (size_t)8 * 42 * 7 * 1024;
constexpr size_t G_OFF = WB_OFF + WB_BYTES;
constexpr size_t ST_OFF = (size_t)64 << 20;

constexpr int PQ_NS = 5;
constexpr int PQ_SLOT = 30720;
constexpr int H_XB = 12288;
constexpr int H_WOFF = 8 * H_XB;
constexpr int H_WSLOT = 14336;
constexpr int K1_LDS = H_WOFF + 4 * H_WSLOT;

#define STAMP(i) do {} while (0)
#define RSTAMP(i) do {} while (0)

template <int IMM>
__device__ __forceinline__ void glds(unsigned voff, const char* sbase, unsigned ldsbase) {
  asm volatile("s_add_u32 m0, %2, %3\n\ts_nop 0\n\tglobal_load_lds_dwordx4 %0, %1"
               :: "v"(voff), "s"(sbase), "s"(ldsbase), "i"(IMM) : "memory", "scc");
}
template <int N> __device__ __forceinline__ void wait_vm() { asm volatile("s_waitcnt vmcnt(%0)" :: "i"(N) : "memory"); }
template <int N> __device__ __forceinline__ void wait_vm_bar() {
  asm volatile("s_waitcnt vmcnt(%0)\n\ts_barrier" :: "i"(N) : "memory");
}
__device__ __forceinline__ void wait_lgkm_bar() { asm volatile("s_waitcnt lgkmcnt(0)\n\ts_barrier" ::: "memory"); }
__device__ __forceinline__ void bar() { asm volatile("s_barrier" ::: "memory"); }
#define SCHED0 __builtin_amdgcn_sched_barrier(0)

__device__ __forceinline__ bf16x8 lds_b128(unsigned addr) { return *(LDS3 bf16x8*)(addr); }
__device__ __forceinline__ bf16x8 lds_tr2(unsigned addr) {
  bf16x4 lo = __builtin_amdgcn_ds_read_tr16_b64_v4bf16((LDS3 bf16x4*)(addr));
  bf16x4 hi = __builtin_amdgcn_ds_read_tr16_b64_v4bf16((LDS3 bf16x4*)(addr + 16 * 224));
  return __builtin_shufflevector(lo, hi, 0, 1, 2, 3, 4, 5, 6, 7);
}
__device__ __forceinline__ f32x4 mfma(bf16x8 a, bf16x8 b, f32x4 c) {
  return __builtin_amdgcn_mfma_f32_16x16x32_bf16(a, b, c, 0, 0, 0);
}
__device__ __forceinline__ unsigned pk(float a, float b) {
  f32x2 v = {a, b};
  bf16x2 p = __builtin_convertvector(v, bf16x2);
  return __builtin_bit_cast(unsigned, p);
}
__device__ __forceinline__ float sigmoidf_(float z) {
  return __builtin_amdgcn_rcpf(1.0f + __builtin_amdgcn_exp2f(-1.4426950408889634f * z));
}
__device__ __forceinline__ int perm32(int p) { return 8 * ((p & 15) >> 2) + (p & 3) + 4 * (p >> 4); }

template <int NX>
__device__ __forceinline__ void pq_issue(const unsigned (&vC)[4], const unsigned (&vX)[4], const char* cb,
                                         const char* xb, unsigned ldsC, unsigned ldsX) {
  glds<0>(vC[0], cb, ldsC);
  glds<1024>(vC[1], cb, ldsC);
  glds<2048>(vC[2], cb, ldsC);
  glds<3072>(vC[3], cb, ldsC);
  glds<0>(vX[0], xb, ldsX);
  glds<1024>(vX[1], xb, ldsX);
  glds<2048>(vX[2], xb, ldsX);
  if (NX == 4) glds<3072>(vX[3], xb, ldsX);
}

template <int NX>
__device__ __forceinline__ void pq_loader(const char* cr, const char* ci, const char* x, unsigned lds0, int tb, int ds,
                                          int lw, int lane) {
  constexpr int PER = 4 + NX;
  unsigned vC[4], vX[4];
#pragma unroll
  for (int qq = 0; qq < 4; ++qq) {
    int row = 64 * tb + 32 * (lw & 1) + 8 * qq + (lane >> 3);
    int c = (lane & 7) ^ ((4 * (qq & 1) + (lane >> 4)) & 7);
    vC[qq] = row * 4096 + c * 16;
  }
  const int q0 = (NX == 4) ? 4 * lw : 8 + 3 * (lw - 2);
#pragma unroll
  for (int qq = 0; qq < 4; ++qq) {
    int o = 1024 * (q0 + qq) + 16 * lane;
    int pos = o / 224, colb = o - 224 * pos;
    int s = (pos & 32) + perm32(pos & 31);
    vX[qq] = s * 896 + ds * 224 + colb;
  }
  const char* cb = (lw >> 1) ? ci : cr;
  const char* xb = x;
  const unsigned ldsC = lds0 + lw * 4096, ldsX = lds0 + 16384 + q0 * 1024;
  asm volatile("s_nop 4" ::: "memory");
#pragma unroll
  for (int i = 0; i < PQ_NS; ++i) {
    pq_issue<NX>(vC, vX, cb, xb, ldsC + i * PQ_SLOT, ldsX + i * PQ_SLOT);
    cb += 128;
    xb += 64 * 896;
  }
  wait_vm_bar<PER*(PQ_NS - 1)>();
  unsigned so = 0;
  for (int i = 0; i < 32 - PQ_NS; ++i) {
    wait_vm_bar<PER*(PQ_NS - 2)>();
    pq_issue<NX>(vC, vX, cb, xb, ldsC + so, ldsX + so);
    cb += 128;
    xb += 64 * 896;
    so = (so == (PQ_NS - 1) * PQ_SLOT) ? 0 : so + PQ_SLOT;
  }
  wait_vm_bar<PER * 3>();
  wait_vm_bar<PER * 2>();
  wait_vm_bar<PER * 1>();
  wait_vm_bar<0>();
}

struct PqFrag { bf16x8 a[7]; bf16x8 b[2]; };

template <int KK>
__device__ __forceinline__ void pq_read(PqFrag& f, unsigned vx, unsigned vc) {
  f.b[0] = lds_b128((vc ^ (KK * 64)));
  f.b[1] = lds_b128((vc ^ (KK * 64)) + 8192);
#pragma unroll
  for (int j = 0; j < 7; ++j) f.a[j] = lds_tr2(vx + KK * 7168 + 32 * j);
}
__device__ __forceinline__ void pq_mma(f32x4 (&acc)[7][2], const PqFrag& f) {
#pragma unroll
  for (int j = 0; j < 7; ++j) {
    acc[j][0] = mfma(f.a[j], f.b[0], acc[j][0]);
    acc[j][1] = mfma(f.a[j], f.b[1], acc[j][1]);
  }
}
__device__ __forceinline__ void pq_pattern() {
#pragma unroll
  for (int j = 0; j < 8; ++j) {
    __builtin_amdgcn_sched_group_barrier(0x100, 2, 0);
    __builtin_amdgcn_sched_group_barrier(0x8, 1, 0);
  }
  __builtin_amdgcn_sched_group_barrier(0x8, 6, 0);
}

__device__ __forceinline__ void pq_consumer(char* hb, unsigned lds0, int tb, int ds, int cw, int lane, u64* stp) {
  const int wv = cw;
  const int g = lane >> 4, i16 = lane & 15;
  const unsigned vx0 = lds0 + 16384 + (4 * g + (i16 >> 2)) * 224 + 8 * (lane & 3);
  const unsigned vc0 = lds0 + (16 * cw + i16) * 128 + ((g ^ (i16 >> 1)) << 4);
  f32x4 acc[7][2];
#pragma unroll
  for (int j = 0; j < 7; ++j) { acc[j][0] = f32x4{0, 0, 0, 0}; acc[j][1] = f32x4{0, 0, 0, 0}; }
  PqFrag f0, f1;
  bar();
  STAMP(1);
  pq_read<0>(f0, vx0, vc0);
  unsigned so = 0;
  for (int i = 0; i < 31; ++i) {
    SCHED0;
    pq_read<1>(f1, vx0 + so, vc0 + so);
    pq_mma(acc, f0);
    pq_pattern();
    SCHED0;
    wait_lgkm_bar();
    so = (so == (PQ_NS - 1) * PQ_SLOT) ? 0 : so + PQ_SLOT;
    SCHED0;
    pq_read<0>(f0, vx0 + so, vc0 + so);
    pq_mma(acc, f1);
    pq_pattern();
  }
  SCHED0;
  pq_read<1>(f1, vx0 + so, vc0 + so);
  pq_mma(acc, f0);
  pq_pattern();
  SCHED0;
  pq_mma(acc, f1);
  STAMP(2);
  char* dst = hb + ((size_t)(tb * NK2 + 7 * ds) * 4 + cw) * 1024 + lane * 16;
#pragma unroll
  for (int j = 0; j < 7; ++j) {
    u32x4 v = {pk(acc[j][0][0], acc[j][0][1]), pk(acc[j][0][2], acc[j][0][3]),
               pk(acc[j][1][0], acc[j][1][1]), pk(acc[j][1][2], acc[j][1][3])};
    *(u32x4*)(dst + j * 4096) = v;
  }
}

struct HAddr { unsigned vW[2]; unsigned vX[4]; const char* w0; const char* w1; const char* x; unsigned lW; unsigned lX; };

template <int U>
__device__ __forceinline__ void h_issue_w(const HAddr& a) {
  glds<(U & 3) * H_WSLOT>(a.vW[0], a.w0 + (size_t)U * (32 * 3584), a.lW);
  glds<(U & 3) * H_WSLOT + 1024>(a.vW[1], a.w1 + (size_t)U * (32 * 3584), a.lW);
}
template <int J>
__device__ __forceinline__ void h_issue_x(const HAddr& a) {
  glds<(J % 3) * 4096>(a.vX[0], a.x + J * 128, a.lX);
  glds<(J % 3) * 4096 + 1024>(a.vX[1], a.x + J * 128, a.lX);
  glds<(J % 3) * 4096 + 2048>(a.vX[2], a.x + J * 128, a.lX);
  glds<(J % 3) * 4096 + 3072>(a.vX[3], a.x + J * 128, a.lX);
}
template <int U, int N>
__device__ __forceinline__ bf16x8 h_read_a(unsigned vw) {
  return lds_tr2(vw + (U & 3) * H_WSLOT + (N / 7) * 7168 + (N % 7) * 32);
}
template <int U>
__device__ __forceinline__ void h_read_b(bf16x8 (&b)[2], unsigned vx) {
  constexpr int J = U >> 1, KK = U & 1;
  b[0] = lds_b128((vx ^ (KK * 64)) + (J % 3) * 4096);
  b[1] = lds_b128((vx ^ (KK * 64)) + (J % 3) * 4096 + 2048);
}

constexpr int H_PD = 6;

template <int U, int N>
__device__ __forceinline__ void h_frag(f32x4 (&acc)[14][2], bf16x8 (&a)[7], bf16x8 (&bc)[2], bf16x8 (&bn)[2],
                                       const HAddr& ad, unsigned vw, unsigned vx) {
  if constexpr (N == 7 && U < 13) {
    SCHED0;
    constexpr int W = (U <= 10) ? 6 : (U == 11 ? 2 : 0);
    wait_vm_bar<W>();
    if constexpr (U + 3 <= 13) h_issue_w<U + 3>(ad);
    if constexpr ((U & 1) == 0 && U / 2 + 2 <= 6) h_issue_x<U / 2 + 2>(ad);
    SCHED0;
    h_read_b<U + 1>(bn, vx);
  }
  bf16x8 cur = a[N % 7];
  acc[N][0] = mfma(cur, bc[0], acc[N][0]);
  acc[N][1] = mfma(cur, bc[1], acc[N][1]);
  if constexpr (N + 7 < 14) a[N % 7] = h_read_a<U, N + 7>(vw);
  else if constexpr (U < 13) a[N % 7] = h_read_a<U + 1, N - 7>(vw);
}
template <int U, int N>
__device__ __forceinline__ void h_frags(f32x4 (&acc)[14][2], bf16x8 (&a)[7], bf16x8 (&bc)[2], bf16x8 (&bn)[2],
                                        const HAddr& ad, unsigned vw, unsigned vx) {
  h_frag<U, N>(acc, a, bc, bn, ad, vw, vx);
  if constexpr (N < 13) h_frags<U, N + 1>(acc, a, bc, bn, ad, vw, vx);
}
template <int U>
__device__ __forceinline__ void h_steps(f32x4 (&acc)[14][2], bf16x8 (&a)[7], bf16x8 (&b0)[2], bf16x8 (&b1)[2],
                                        const HAddr& ad, unsigned vw, unsigned vx) {
  if constexpr ((U & 1) == 0) h_frags<U, 0>(acc, a, b0, b1, ad, vw, vx);
  else h_frags<U, 0>(acc, a, b1, b0, ad, vw, vx);
  if constexpr (U < 13) h_steps<U + 1>(acc, a, b0, b1, ad, vw, vx);
}

struct WJob { u32x4 v; };

__device__ __forceinline__ void w2_bgather(const u16* wd, int q, int lane, unsigned (&r)[8]) {
  const int g = lane >> 4, i16 = lane & 15;
  const int c = q / 196, rem = q % 196, pp = rem / 7, sl = rem % 7;
  const int kp = 2 * pp + (sl < 6 ? sl / 3 : (i16 >> 3));
  const int e = 56 * c + (sl < 6 ? 16 * (sl % 3) + i16 : 48 + (i16 & 7));
  const int sp = kp / 7, r7 = kp % 7;
  const int fa = 14 * sp + (r7 < 3 ? 2 * r7 : (r7 < 6 ? 7 + 2 * (r7 - 3) : 6));
  const int fb = r7 < 6 ? fa + 1 : 14 * sp + 13;
  const u16* pa = wd + (16 * fa + 4 * g) * D + e;
  const u16* pb = wd + (16 * fb + 4 * g) * D + e;
#pragma unroll
  for (int j = 0; j < 4; ++j) { r[j] = pa[j * D]; r[4 + j] = pb[j * D]; }
}
__device__ __forceinline__ void w2_bstore(char* wb, int q, int lane, const unsigned (&r)[8]) {
  const int c = q / 196, rem = q % 196;
  u32x4 v = {r[0] | (r[1] << 16), r[2] | (r[3] << 16), r[4] | (r[5] << 16), r[6] | (r[7] << 16)};
  *(u32x4*)(wb + ((size_t)(c * 42 + 14) * 7 + rem) * 1024 + lane * 16) = v;
}
__device__ __forceinline__ void w2_agather(const u16* wre, const u16* wim, int q, int lane, u32x2& lo, u32x2& hi) {
  const int g = lane >> 4, i16 = lane & 15;
  const int c = q / 98, rem = q % 98, pp = rem / 7, sl = rem % 7;
  const int kp = 2 * pp + (sl < 6 ? sl / 3 : (i16 >> 3));
  const int e = 56 * c + (sl < 6 ? 16 * (sl % 3) + i16 : 48 + (i16 & 7));
  lo = *(const u32x2*)(wre + e * D + 16 * kp + 4 * g);
  hi = *(const u32x2*)(wim + e * D + 16 * kp + 4 * g);
}
__device__ __forceinline__ void w2_astore(char* wb, int q, int lane, u32x2 lo, u32x2 hi) {
  const int c = q / 98, rem = q % 98;
  u32x4 v = {lo[0], lo[1], hi[0] ^ 0x80008000u, hi[1] ^ 0x80008000u};
  *(u32x4*)(wb + ((size_t)(c * 42) * 7 + rem) * 1024 + lane * 16) = v;
}
__device__ __forceinline__ float dot8(u32x4 a, u32x4 b) {
  float s = 0.f;
#pragma unroll
  for (int j = 0; j < 4; ++j) {
    s = __builtin_fmaf(__builtin_bit_cast(float, a[j] << 16), __builtin_bit_cast(float, b[j] << 16), s);
    s = __builtin_fmaf(__builtin_bit_cast(float, a[j] & 0xffff0000u), __builtin_bit_cast(float, b[j] & 0xffff0000u), s);
  }
  return s;
}

__device__ __forceinline__ void h_task(const char* x, const char* wg, const char* wu, const u16* wre, const u16* wim,
                                       const u16* wd, const char* gw, char* ws, unsigned lds0, int fs, int rb, int w,
                                       int lane, u64* stp) {
  const int wv = w;
  const int g = lane >> 4, i16 = lane & 15;
  HAddr ad;
  {
    const int q0 = (w < 7) ? 2 * w : 12;
#pragma unroll
    for (int qq = 0; qq < 2; ++qq) {
      int q = q0 + qq;
      int ql = q >= 7 ? q - 7 : q;
      int o = 1024 * ql + 16 * lane;
      int pos = o / 224, colb = o - 224 * pos;
      ad.vW[qq] = perm32(pos) * 3584 + fs * 224 + colb;
    }
    ad.w0 = (q0 >= 7) ? wu : wg;
    ad.w1 = (q0 + 1 >= 7) ? wu : wg;
    ad.lW = lds0 + H_WOFF + q0 * 1024;
#pragma unroll
    for (int qq = 0; qq < 4; ++qq) {
      int row = 256 * rb + 32 * w + 8 * qq + (lane >> 3);
      int c = (lane & 7) ^ ((4 * (qq & 1) + (lane >> 4)) & 7);
      ad.vX[qq] = row * 896 + c * 16;
    }
    ad.x = x;
    ad.lX = lds0 + w * H_XB;
  }
  const unsigned vw = lds0 + H_WOFF + (4 * g + (i16 >> 2)) * 224 + 8 * (lane & 3);
  const unsigned vx = lds0 + w * H_XB + i16 * 128 + ((g ^ (i16 >> 1)) << 4);
  asm volatile("s_nop 4" ::: "memory");
  h_issue_w<0>(ad);
  h_issue_x<0>(ad);
  h_issue_w<1>(ad);
  h_issue_w<2>(ad);
  h_issue_x<1>(ad);

  f32x4 acc[14][2];
#pragma unroll
  for (int n = 0; n < 14; ++n) { acc[n][0] = f32x4{0, 0, 0, 0}; acc[n][1] = f32x4{0, 0, 0, 0}; }
  bf16x8 a[7], b0[2], b1[2];
  wait_vm_bar<8>();
  STAMP(1);
  h_read_b<0>(b0, vx);
  a[0] = h_read_a<0, 0>(vw); a[1] = h_read_a<0, 1>(vw); a[2] = h_read_a<0, 2>(vw); a[3] = h_read_a<0, 3>(vw);
  a[4] = h_read_a<0, 4>(vw); a[5] = h_read_a<0, 5>(vw); a[6] = h_read_a<0, 6>(vw);
  h_steps<0>(acc, a, b0, b1, ad, vw, vx);
  SCHED0;
  STAMP(2);

  char* wb = ws + WB_OFF;
  const int wgid = (fs * 8 + rb) * 8 + w;
  unsigned r0[8], r1[8];
  u32x2 alo = {0, 0}, ahi = {0, 0};
  w2_bgather(wd, wgid, lane, r0);
  const bool two_b = wgid < 544;
  const bool has_a = wgid >= 544 || wgid < 304;
  const int qa = wgid >= 544 ? wgid - 544 : wgid + 480;
  if (two_b) w2_bgather(wd, wgid + 1024, lane, r1);
  if (has_a) w2_agather(wre, wim, qa, lane, alo, ahi);
  const int grow = 256 * rb + 16 * fs + 2 * w;
  u32x4 gx0 = {0, 0, 0, 0}, gx1 = {0, 0, 0, 0}, gv = {0, 0, 0, 0};
  if (lane < 56) {
    gx0 = *(const u32x4*)(x + grow * 896 + lane * 16);
    gx1 = *(const u32x4*)(x + (grow + 1) * 896 + lane * 16);
    gv = *(const u32x4*)(gw + lane * 16);
  }

  const int tb = 4 * rb + (w >> 1);
  const int kap = 28 + 7 * (fs >> 1);
#pragma unroll
  for (int mt = 0; mt < 2; ++mt) {
    char* dst = ws + ((size_t)(tb * NK2 + kap) * 4 + 2 * (w & 1) + mt) * 1024 + lane * 16;
    unsigned hp[7][2];
#pragma unroll
    for (int j = 0; j < 7; ++j) {
      float h[4];
#pragma unroll
      for (int r = 0; r < 4; ++r) h[r] = acc[7 + j][mt][r] * sigmoidf_(acc[j][mt][r]);
      hp[j][0] = pk(h[0], h[1]);
      hp[j][1] = pk(h[2], h[3]);
    }
#pragma unroll
    for (int jj = 0; jj < 3; ++jj) {
      u32x4 v = {hp[2 * jj][0], hp[2 * jj][1], hp[2 * jj + 1][0], hp[2 * jj + 1][1]};
      *(u32x4*)(dst + (size_t)(3 * (fs & 1) + jj) * 4096) = v;
    }
    u32x2 v6 = {hp[6][0], hp[6][1]};
    *(u32x2*)(dst + (size_t)6 * 4096 + 8 * (fs & 1)) = v6;
  }
  STAMP(3);

  w2_bstore(wb, wgid, lane, r0);
  if (two_b) w2_bstore(wb, wgid + 1024, lane, r1);
  if (has_a) w2_astore(wb, qa, lane, alo, ahi);
  float z0 = dot8(gx0, gv), z1 = dot8(gx1, gv);
#pragma unroll
  for (int o = 32; o >= 1; o >>= 1) { z0 += __shfl_xor(z0, o); z1 += __shfl_xor(z1, o); }
  if (lane < 2) ((float*)(ws + G_OFF))[grow + lane] = sigmoidf_(lane ? z1 : z0);
  STAMP(4);
}

__global__ __launch_bounds__(512, 2) void k1(const char* __restrict__ x, const char* __restrict__ cr,
                                             const char* __restrict__ ci, const u16* __restrict__ wre,
                                             const u16* __restrict__ wim, const char* __restrict__ wg,
                                             const char* __restrict__ wu, const u16* __restrict__ wd,
                                             const char* __restrict__ gw, char* __restrict__ ws) {
  extern __shared__ __attribute__((aligned(16))) char lds[];
  const unsigned lds0 = (unsigned)(size_t)(LDS3 char*)lds;
  const int lane = threadIdx.x & 63;
  const int w = __builtin_amdgcn_readfirstlane(threadIdx.x >> 6);
  const int xcd = blockIdx.x & 7, idx = blockIdx.x >> 3;
  u64* stp = (u64*)(ws + ST_OFF);
  {
    const int wv = w;
    RSTAMP(8);
    STAMP(0);
  }
  if (idx < 16) {
    const int tb = 4 * xcd + (idx >> 2), ds = idx & 3;
    if (w < 4) pq_consumer(ws, lds0, tb, ds, w, lane, stp);
    else if (w < 6) pq_loader<4>(cr, ci, x, lds0, tb, ds, w - 4, lane);
    else pq_loader<3>(cr, ci, x, lds0, tb, ds, w - 4, lane);
  } else {
    const int i2 = idx - 16;
    h_task(x, wg, wu, wre, wim, wd, gw, ws, lds0, 2 * xcd + (i2 >> 3), i2 & 7, w, lane, stp);
  }
  {
    const int wv = w;
    RSTAMP(9);
  }
}

constexpr int K2_NW = 6;
constexpr int K2_PP = 42 / K2_NW;
constexpr int K2_RS = 68;
constexpr int K2_LDS = K2_NW * 64 * K2_RS * 4;

struct K2Frag { bf16x8 h[2][4]; bf16x8 w[7]; };

__device__ __forceinline__ void k2_load(K2Frag& f, const char* hp, const char* wp, unsigned vo) {
#pragma unroll
  for (int s = 0; s < 2; ++s)
#pragma unroll
    for (int m = 0; m < 4; ++m) f.h[s][m] = *(const bf16x8*)(hp + s * 4096 + m * 1024 + vo);
#pragma unroll
  for (int n = 0; n < 7; ++n) f.w[n] = *(const bf16x8*)(wp + n * 1024 + vo);
}
__device__ __forceinline__ void k2_mma(f32x4 (&acc)[5][4], const K2Frag& f) {
#pragma unroll
  for (int s = 0; s < 2; ++s) {
#pragma unroll
    for (int n = 0; n < 3; ++n)
#pragma unroll
      for (int m = 0; m < 4; ++m) acc[n][m] = mfma(f.w[3 * s + n], f.h[s][m], acc[n][m]);
#pragma unroll
    for (int m = 0; m < 4; ++m) acc[3 + s][m] = mfma(f.w[6], f.h[s][m], acc[3 + s][m]);
  }
}

__global__ __launch_bounds__(K2_NW * 64) void k2(const char* __restrict__ ws, u16* __restrict__ out, u64* stp) {
  extern __shared__ __attribute__((aligned(16))) char lds[];
  const int lane = threadIdx.x & 63;
  const int w = __builtin_amdgcn_readfirstlane(threadIdx.x >> 6);
  const int wv = w;
  RSTAMP(10);
  STAMP(11);
  const int xcd = blockIdx.x & 7, idx = blockIdx.x >> 3;
  const int tb = 4 * xcd + (idx >> 3), c = idx & 7;
  const char* hp = ws + (size_t)(tb * NK2 + 2 * K2_PP * w) * 4096;
  const char* wp = ws + WB_OFF + (size_t)(c * 42 + K2_PP * w) * 7168;
  const unsigned vo = lane * 16;
  const int g = lane >> 4, i16 = lane & 15;

  f32x4 acc[5][4];
#pragma unroll
  for (int n = 0; n < 5; ++n)
#pragma unroll
    for (int m = 0; m < 4; ++m) acc[n][m] = f32x4{0, 0, 0, 0};
  K2Frag f[2];
  k2_load(f[0], hp, wp, vo);
  k2_load(f[1], hp + 8192, wp + 7168, vo);
  float gt[4];
#pragma unroll
  for (int m = 0; m < 4; ++m) gt[m] = ((const float*)(ws + G_OFF))[64 * tb + 16 * m + i16];
#pragma unroll
  for (int p = 0; p < K2_PP; ++p) {
    k2_mma(acc, f[p & 1]);
    if (p + 2 < K2_PP) k2_load(f[p & 1], hp + (p + 2) * 8192, wp + (p + 2) * 7168, vo);
  }
  STAMP(12);
  const bool isA = w < K2_NW / 3;
  LDS3 char* red = (LDS3 char*)lds + w * (64 * K2_RS * 4);
#pragma unroll
  for (int m = 0; m < 4; ++m) {
    const float sc = isA ? gt[m] : 1.0f - gt[m];
#pragma unroll
    for (int n = 0; n < 4; ++n) {
      f32x4 v = (n < 3) ? acc[n][m] : (g < 2 ? acc[3][m] : acc[4][m]);
      v *= sc;
      *(LDS3 f32x4*)(red + ((16 * m + i16) * K2_RS + 16 * n + 4 * g) * 4) = v;
    }
  }
  __syncthreads();
  for (int it = threadIdx.x; it < 448; it += K2_NW * 64) {
    const int t = it / 7, j = it - 7 * t;
    f32x4 s0 = {0, 0, 0, 0}, s1 = {0, 0, 0, 0};
#pragma unroll
    for (int ww = 0; ww < K2_NW; ++ww) {
      LDS3 char* p = (LDS3 char*)lds + ww * (64 * K2_RS * 4) + (t * K2_RS + 8 * j) * 4;
      s0 += *(LDS3 f32x4*)p;
      s1 += *(LDS3 f32x4*)(p + 16);
      if (j == 6) { s0 += *(LDS3 f32x4*)(p + 32); s1 += *(LDS3 f32x4*)(p + 48); }
    }
    u32x4 v = {pk(s0[0], s0[1]), pk(s0[2], s0[3]), pk(s1[0], s1[1]), pk(s1[2], s1[3])};
    *(u32x4*)(out + (size_t)(64 * tb + t) * D + 56 * c + 8 * j) = v;
  }
  STAMP(13);
  RSTAMP(14);
}

}

extern "C" void kernel_launch(void* const* d_in, const int* in_sizes, int n_in, void* d_out, int out_size,
                              void* d_ws, size_t ws_size, hipStream_t stream) {
  (void)in_sizes; (void)n_in; (void)out_size; (void)ws_size;
  (void)hipFuncSetAttribute((const void*)k1, hipFuncAttributeMaxDynamicSharedMemorySize, K1_LDS);
  (void)hipFuncSetAttribute((const void*)k2, hipFuncAttributeMaxDynamicSharedMemorySize, K2_LDS);
  k1<<<256, 512, K1_LDS, stream>>>((const char*)d_in[0], (const char*)d_in[1], (const char*)d_in[2],
                                   (const u16*)d_in[3], (const u16*)d_in[4], (const char*)d_in[6],
                                   (const char*)d_in[7], (const u16*)d_in[8], (const char*)d_in[9], (char*)d_ws);
  k2<<<256, K2_NW * 64, K2_LDS, stream>>>((const char*)d_ws, (u16*)d_out, (u64*)((char*)d_ws + ST_OFF));
}
```

```cpp
#include <hip/hip_runtime.h>
#include <stdint.h>

namespace {

typedef unsigned short u16;
typedef unsigned long long u64;
typedef __attribute__((ext_vector_type(8))) __bf16 bf16x8;
typedef __attribute__((ext_vector_type(4))) __bf16 bf16x4;
typedef __attribute__((ext_vector_type(2))) __bf16 bf16x2;
typedef __attribute__((ext_vector_type(4))) float f32x4;
typedef __attribute__((ext_vector_type(2))) float f32x2;
typedef __attribute__((ext_vector_type(4))) unsigned u32x4;
typedef __attribute__((ext_vector_type(2))) unsigned u32x2;
#define LDS3 __attribute__((address_space(3)))

constexpr int S = 2048, D = 448, F = 1792;
constexpr int NK2 = 84;
constexpr size_t HB_BYTES = (size_t)32 * NK2 * 4096;
constexpr size_t WB_OFF = HB_BYTES;
constexpr size_t WB_BYTES = (size_t)8 * 42 * 7 * 1024;
constexpr size_t G_OFF = WB_OFF + WB_BYTES;
constexpr size_t ST_OFF = (size_t)64 << 20;

#define PQ_KSTART(tb, ds) ((8 * (((ds) + (tb)) & 3) + ((tb) >> 2)) & 31)
#define H_PIN 1
#define K2_PIN 1
constexpr int PQ_NS = 5;
constexpr int H_XSLOT = 32768;
constexpr int H_WOFF = 3 * H_XSLOT;
constexpr int H_WSLOT = 16384;
constexpr int K1_LDS = H_WOFF + 4 * H_WSLOT;

#define STAMP(i) do {} while (0)
#define RSTAMP(i) do {} while (0)

template <int IMM>
__device__ __forceinline__ void glds(unsigned voff, const char* sbase, unsigned ldsbase) {
  asm volatile("s_add_u32 m0, %2, %3\n\ts_nop 0\n\tglobal_load_lds_dwordx4 %0, %1"
               :: "v"(voff), "s"(sbase), "s"(ldsbase), "i"(IMM) : "memory", "scc");
}
#define C_MOD ""
template <int IMM>
__device__ __forceinline__ void glds_c(unsigned voff, const char* sbase, unsigned ldsbase) {
  asm volatile("s_add_u32 m0, %2, %3\n\ts_nop 0\n\tglobal_load_lds_dwordx4 %0, %1" C_MOD
               :: "v"(voff), "s"(sbase), "s"(ldsbase), "i"(IMM) : "memory", "scc");
}
template <int N> __device__ __forceinline__ void wait_vm() { asm volatile("s_waitcnt vmcnt(%0)" :: "i"(N) : "memory"); }
template <int N> __device__ __forceinline__ void wait_vm_bar() {
  asm volatile("s_waitcnt vmcnt(%0)\n\ts_barrier" :: "i"(N) : "memory");
}
__device__ __forceinline__ void wait_lgkm_bar() { asm volatile("s_waitcnt lgkmcnt(0)\n\ts_barrier" ::: "memory"); }
__device__ __forceinline__ void bar() { asm volatile("s_barrier" ::: "memory"); }
#define SCHED0 __builtin_amdgcn_sched_barrier(0)

__device__ __forceinline__ bf16x8 lds_b128(unsigned addr) { return *(LDS3 bf16x8*)(addr); }
template <int HI>
__device__ __forceinline__ bf16x8 lds_tr2(unsigned addr) {
  bf16x4 lo = __builtin_amdgcn_ds_read_tr16_b64_v4bf16((LDS3 bf16x4*)(addr));
  bf16x4 hi = __builtin_amdgcn_ds_read_tr16_b64_v4bf16((LDS3 bf16x4*)(addr + HI));
  return __builtin_shufflevector(lo, hi, 0, 1, 2, 3, 4, 5, 6, 7);
}
__device__ __forceinline__ f32x4 mfma(bf16x8 a, bf16x8 b, f32x4 c) {
  return __builtin_amdgcn_mfma_f32_16x16x32_bf16(a, b, c, 0, 0, 0);
}
__device__ __forceinline__ unsigned pk(float a, float b) {
  f32x2 v = {a, b};
  bf16x2 p = __builtin_convertvector(v, bf16x2);
  return __builtin_bit_cast(unsigned, p);
}
__device__ __forceinline__ float sigmoidf_(float z) {
  return __builtin_amdgcn_rcpf(1.0f + __builtin_amdgcn_exp2f(-1.4426950408889634f * z));
}
__device__ __forceinline__ int perm32(int p) { return 8 * ((p & 15) >> 2) + (p & 3) + 4 * (p >> 4); }

template <int NJ> struct Pq {
  static constexpr int ROWB = NJ * 32;
  static constexpr int SLOT = 16384 + 64 * ROWB;
  static constexpr int NX = NJ / 2;
  static constexpr int PER = 4 + NX;
  static constexpr int NH = NJ / 2;
  __device__ static __forceinline__ int swz(int pos) { return NJ == 8 ? (pos & 7) : ((pos >> 1) & 3); }
};

template <int NJ>
__device__ __forceinline__ void pq_loader(const char* cr, const char* ci, const char* x, unsigned lds0, int tb, int ds,
                                          int lw, int lane) {
  using P = Pq<NJ>;
  unsigned vC[4], vX[P::NX];
#pragma unroll
  for (int qq = 0; qq < 4; ++qq) {
    int row = 64 * tb + 32 * (lw & 1) + 8 * qq + (lane >> 3);
    int c = (lane & 7) ^ ((4 * (qq & 1) + (lane >> 4)) & 7);
    vC[qq] = row * 4096 + c * 16;
  }
#pragma unroll
  for (int qq = 0; qq < P::NX; ++qq) {
    constexpr int LPR = P::ROWB / 16;
    int pos = (P::NX * lw + qq) * (64 / LPR) + lane / LPR;
    int c = lane % LPR;
    int s = (pos & 32) + perm32(pos & 31);
    vX[qq] = s * 896 + ds * 256 + (((c >> 1) ^ P::swz(pos)) << 5) + ((c & 1) << 4);
  }
  const char* cb0 = (lw >> 1) ? ci : cr;
  const unsigned ldsC = lds0 + lw * 4096, ldsX = lds0 + 16384 + P::NX * lw * 1024;
  int kk = PQ_KSTART(tb, ds);
  asm volatile("s_nop 4" ::: "memory");
  auto issue = [&](unsigned so) {
    const char* cb = cb0 + kk * 128;
    const char* xb = x + kk * (64 * 896);
    glds_c<0>(vC[0], cb, ldsC + so);
    glds_c<1024>(vC[1], cb, ldsC + so);
    glds_c<2048>(vC[2], cb, ldsC + so);
    glds_c<3072>(vC[3], cb, ldsC + so);
    glds<0>(vX[0], xb, ldsX + so);
    glds<1024>(vX[1], xb, ldsX + so);
    if constexpr (P::NX == 4) {
      glds<2048>(vX[2], xb, ldsX + so);
      glds<3072>(vX[3], xb, ldsX + so);
    }
    kk = (kk + 1) & 31;
  };
  issue(0);
  issue(P::SLOT);
  wait_vm_bar<P::PER>();
  issue(2 * P::SLOT);
  issue(3 * P::SLOT);
  issue(4 * P::SLOT);
  unsigned so = 0;
  for (int i = 0; i < 32 - PQ_NS; ++i) {
    wait_vm_bar<P::PER*(PQ_NS - 2)>();
    issue(so);
    so = (so == (PQ_NS - 1) * P::SLOT) ? 0 : so + P::SLOT;
  }
  wait_vm_bar<P::PER * 3>();
  wait_vm_bar<P::PER * 2>();
  wait_vm_bar<P::PER * 1>();
  wait_vm_bar<0>();
}

template <int NH> struct PqFrag { bf16x8 a[NH]; bf16x8 b[4]; };

template <int NJ, int KK>
__device__ __forceinline__ void pq_read(PqFrag<NJ / 2>& f, const unsigned (&vx)[NJ / 2], unsigned vc, unsigned so) {
  constexpr int RB = Pq<NJ>::ROWB;
  const unsigned c = (vc ^ (KK * 64)) + so;
  f.b[0] = lds_b128(c);
  f.a[0] = lds_tr2<16 * RB>(vx[0] + so + KK * 32 * RB);
  f.b[1] = lds_b128(c + 8192);
  f.b[2] = lds_b128(c + 2048);
  f.b[3] = lds_b128(c + 8192 + 2048);
#pragma unroll
  for (int j = 1; j < NJ / 2; ++j) f.a[j] = lds_tr2<16 * RB>(vx[j] + so + KK * 32 * RB);
}
template <int NH>
__device__ __forceinline__ void pq_mma(f32x4 (&acc)[NH][4], const PqFrag<NH>& f) {
#pragma unroll
  for (int j = 0; j < NH; ++j)
#pragma unroll
    for (int m = 0; m < 4; ++m) acc[j][m] = mfma(f.a[j], f.b[m], acc[j][m]);
}
template <int NH>
__device__ __forceinline__ void pq_pattern() {
  constexpr int NR = 4 + 2 * NH, NM = 4 * NH;
  constexpr int DBL = (NH == 4) ? 2 : 2;
#pragma unroll
  for (int j = 0; j < DBL; ++j) {
    __builtin_amdgcn_sched_group_barrier(0x100, 2, 0);
    __builtin_amdgcn_sched_group_barrier(0x8, 1, 0);
  }
#pragma unroll
  for (int j = 0; j < NR - 2 * DBL; ++j) {
    __builtin_amdgcn_sched_group_barrier(0x100, 1, 0);
    __builtin_amdgcn_sched_group_barrier(0x8, 1, 0);
  }
  __builtin_amdgcn_sched_group_barrier(0x8, NM - (NR - DBL), 0);
}

template <int NJ>
__device__ __forceinline__ void pq_consumer(char* hb, unsigned lds0, int tb, int ds, int cw, int lane, u64* stp) {
  using P = Pq<NJ>;
  constexpr int NH = P::NH;
  const int wv = cw;
  const int g = lane >> 4, i16 = lane & 15;
  const int wm = cw & 1, wn = cw >> 1;
  const int pos = 4 * g + (i16 >> 2);
  unsigned vx[NH];
#pragma unroll
  for (int j = 0; j < NH; ++j)
    vx[j] = lds0 + 16384 + pos * P::ROWB + (((NH * wn + j) ^ P::swz(pos)) << 5) + 8 * (lane & 3);
  const unsigned vc = lds0 + (32 * wm + i16) * 128 + ((g ^ (i16 >> 1)) << 4);
  f32x4 acc[NH][4];
#pragma unroll
  for (int j = 0; j < NH; ++j)
#pragma unroll
    for (int m = 0; m < 4; ++m) acc[j][m] = f32x4{0, 0, 0, 0};
  PqFrag<NH> f0, f1;
  bar();
  STAMP(1);
  pq_read<NJ, 0>(f0, vx, vc, 0);
  unsigned so = 0;
  for (int i = 0; i < 31; ++i) {
    SCHED0;
    pq_read<NJ, 1>(f1, vx, vc, so);
    pq_mma<NH>(acc, f0);
    pq_pattern<NH>();
    SCHED0;
    wait_lgkm_bar();
    so = (so == (PQ_NS - 1) * P::SLOT) ? 0 : so + P::SLOT;
    SCHED0;
    pq_read<NJ, 0>(f0, vx, vc, so);
    pq_mma<NH>(acc, f1);
    pq_pattern<NH>();
  }
  SCHED0;
  pq_read<NJ, 1>(f1, vx, vc, so);
  pq_mma<NH>(acc, f0);
  pq_pattern<NH>();
  SCHED0;
  pq_mma<NH>(acc, f1);
  STAMP(2);
  char* dst = hb + ((size_t)(tb * NK2 + 8 * ds + NH * wn) * 4 + 2 * wm) * 1024 + lane * 16;
#pragma unroll
  for (int j = 0; j < NH; ++j)
#pragma unroll
    for (int mt = 0; mt < 2; ++mt) {
      const f32x4 p = acc[j][2 * mt], q = acc[j][2 * mt + 1];
      u32x4 v = {pk(p[0], p[1]), pk(p[2], p[3]), pk(q[0], q[1]), pk(q[2], q[3])};
      *(u32x4*)(dst + j * 4096 + mt * 1024) = v;
    }
}

template <int NJ>
__device__ __forceinline__ void pq_task(const char* x, const char* cr, const char* ci, char* ws, unsigned lds0, int tb,
                                        int ds, int w, int lane, u64* stp) {
  if (w < 4) pq_consumer<NJ>(ws, lds0, tb, ds, w, lane, stp);
  else pq_loader<NJ>(cr, ci, x, lds0, tb, ds, w - 4, lane);
}

struct HAddr { unsigned vW[2]; unsigned vX[4]; const char* w; const char* x; unsigned lW; unsigned lX; };

template <int U, int Q>
__device__ __forceinline__ void h_issue_w(const HAddr& a) {
  glds<(U & 3) * H_WSLOT + Q * 1024>(a.vW[Q], a.w + (size_t)U * (32 * 3584), a.lW);
}
template <int J, int Q>
__device__ __forceinline__ void h_issue_x(const HAddr& a) {
  glds<(J % 3) * H_XSLOT + Q * 1024>(a.vX[Q], a.x + J * 128, a.lX);
}
template <int U, int I>
__device__ __forceinline__ void h_issue(const HAddr& a) {
  if constexpr (I < 2) {
    if constexpr (U + 3 <= 13) h_issue_w<U + 3, I>(a);
  } else {
    if constexpr (U / 2 + 2 <= 6) h_issue_x<U / 2 + 2, 2 * (U & 1) + I - 2>(a);
  }
}
constexpr int h_window(int U) { return U < 0 ? 6 : (U + 3 <= 13 ? 2 : 0) + (U / 2 + 2 <= 6 ? 2 : 0); }

template <int U, int N>
__device__ __forceinline__ bf16x8 h_read_a(const unsigned (&vw)[4]) {
  return lds_tr2<4096>(vw[N >> 1] + (U & 3) * H_WSLOT + (N & 1) * 8192);
}
template <int U>
__device__ __forceinline__ void h_read_b(bf16x8 (&b)[4], unsigned vx) {
  constexpr int J = U >> 1, KK = U & 1;
  const unsigned base = (vx ^ (KK * 64)) + (J % 3 == 2 ? 2 * H_XSLOT : 0);
#pragma unroll
  for (int m = 0; m < 4; ++m) b[m] = lds_b128(base + (J % 3 == 1 ? H_XSLOT : 0) + m * 2048);
}

template <int G, int U, int N>
__device__ __forceinline__ void h_frag(f32x4 (&acc)[8][4], bf16x8 (&a)[4], bf16x8 (&bc)[4], bf16x8 (&bn)[4],
                                       const HAddr& ad, const unsigned (&vw)[4], unsigned vx) {
  if constexpr (N == 4 && U < 13) {
    SCHED0;
    wait_vm_bar<h_window(U - 1)>();
    SCHED0;
    h_read_b<U + 1>(bn, vx);
  }
  const bf16x8 cur = a[N & 3];
#pragma unroll
  for (int m = 0; m < 4; ++m) acc[N][m] = mfma(cur, bc[m], acc[N][m]);
  if constexpr (N + 4 < 8) a[N & 3] = h_read_a<U, N + 4>(vw);
  else if constexpr (U < 13) a[N & 3] = h_read_a<U + 1, N - 4>(vw);
  if constexpr ((N & 1) == G) {
    constexpr int slot = ((N + 4) & 7) >> 1;
    constexpr int win = (N >= 4) ? U : U - 1;
    if constexpr (win >= 0 && win < 13) h_issue<win, slot>(ad);
  }
  SCHED0;
}
template <int G, int U, int N>
__device__ __forceinline__ void h_frags(f32x4 (&acc)[8][4], bf16x8 (&a)[4], bf16x8 (&bc)[4], bf16x8 (&bn)[4],
                                        const HAddr& ad, const unsigned (&vw)[4], unsigned vx) {
  h_frag<G, U, N>(acc, a, bc, bn, ad, vw, vx);
  if constexpr (N < 7) h_frags<G, U, N + 1>(acc, a, bc, bn, ad, vw, vx);
}
template <int G, int U>
__device__ __forceinline__ void h_steps(f32x4 (&acc)[8][4], bf16x8 (&a)[4], bf16x8 (&b0)[4], bf16x8 (&b1)[4],
                                        const HAddr& ad, const unsigned (&vw)[4], unsigned vx) {
  if constexpr ((U & 1) == 0) h_frags<G, U, 0>(acc, a, b0, b1, ad, vw, vx);
  else h_frags<G, U, 0>(acc, a, b1, b0, ad, vw, vx);
  if constexpr (U < 13) h_steps<G, U + 1>(acc, a, b0, b1, ad, vw, vx);
}

__device__ __forceinline__ void h_task(const char* x, const char* wg, const char* wu, char* ws, unsigned lds0, int fb,
                                       int rb, int w, int lane, u64* stp) {
  const int wv = w;
  const int g = lane >> 4, i16 = lane & 15;
  const int wt = w >> 1, wn = w & 1;
  HAddr ad;
#pragma unroll
  for (int qq = 0; qq < 2; ++qq) {
    int pos = 4 * ((2 * w + qq) & 7) + (lane >> 4);
    int c = lane & 15;
    ad.vW[qq] = perm32(pos) * 3584 + fb * 256 + (((c >> 1) ^ (pos & 7)) << 5) + ((c & 1) << 4);
  }
  ad.w = (w < 4) ? wg : wu;
  ad.lW = lds0 + H_WOFF + w * 2048;
#pragma unroll
  for (int qq = 0; qq < 4; ++qq) {
    int row = 256 * rb + 32 * w + 8 * qq + (lane >> 3);
    int c = (lane & 7) ^ ((4 * (qq & 1) + (lane >> 4)) & 7);
    ad.vX[qq] = row * 896 + c * 16;
  }
  ad.x = x;
  ad.lX = lds0 + w * 4096;
  const int pos = 4 * g + (i16 >> 2);
  unsigned vw[4];
#pragma unroll
  for (int j = 0; j < 4; ++j) vw[j] = lds0 + H_WOFF + pos * 256 + (((4 * wn + j) ^ (pos & 7)) << 5) + 8 * (lane & 3);
  const unsigned vx = lds0 + (64 * wt + i16) * 128 + ((g ^ (i16 >> 1)) << 4);
  asm volatile("s_nop 4" ::: "memory");
  h_issue_w<0, 0>(ad); h_issue_w<0, 1>(ad);
  h_issue_x<0, 0>(ad); h_issue_x<0, 1>(ad); h_issue_x<0, 2>(ad); h_issue_x<0, 3>(ad);
  h_issue_w<1, 0>(ad); h_issue_w<1, 1>(ad);

  f32x4 acc[8][4];
#pragma unroll
  for (int n = 0; n < 8; ++n)
#pragma unroll
    for (int m = 0; m < 4; ++m) acc[n][m] = f32x4{0, 0, 0, 0};
  bf16x8 a[4], b0[4], b1[4];
  wait_vm_bar<2>();
  h_issue_w<2, 0>(ad); h_issue_w<2, 1>(ad);
  h_issue_x<1, 0>(ad); h_issue_x<1, 1>(ad); h_issue_x<1, 2>(ad); h_issue_x<1, 3>(ad);
  STAMP(1);
  SCHED0;
  h_read_b<0>(b0, vx);
  a[0] = h_read_a<0, 0>(vw); a[1] = h_read_a<0, 1>(vw); a[2] = h_read_a<0, 2>(vw); a[3] = h_read_a<0, 3>(vw);
  if (w < 4) h_steps<0, 0>(acc, a, b0, b1, ad, vw, vx);
  else h_steps<1, 0>(acc, a, b0, b1, ad, vw, vx);
  SCHED0;
  STAMP(2);

  char* dst = ws + ((size_t)((4 * rb + wt) * NK2 + 28 + 4 * fb + 2 * wn) * 4) * 1024 + lane * 16;
#pragma unroll
  for (int mt = 0; mt < 4; ++mt)
#pragma unroll
    for (int q = 0; q < 2; ++q) {
      unsigned hp[4];
#pragma unroll
      for (int e = 0; e < 2; ++e) {
        const f32x4 ga = acc[4 * q + 2 * e][mt], up = acc[4 * q + 2 * e + 1][mt];
        float h[4];
#pragma unroll
        for (int r = 0; r < 4; ++r) h[r] = up[r] * sigmoidf_(ga[r]);
        hp[2 * e] = pk(h[0], h[1]);
        hp[2 * e + 1] = pk(h[2], h[3]);
      }
      u32x4 v = {hp[0], hp[1], hp[2], hp[3]};
      *(u32x4*)(dst + q * 4096 + mt * 1024) = v;
    }
  STAMP(3);
}

__device__ __forceinline__ void w2_bgather(const u16* wd, int q, int lane, unsigned (&r)[8]) {
  const int g = lane >> 4, i16 = lane & 15;
  const int c = q / 196, rem = q % 196, pp = rem / 7, sl = rem % 7;
  const int kp = 2 * pp + (sl < 6 ? sl / 3 : (i16 >> 3));
  const int e = 56 * c + (sl < 6 ? 16 * (sl % 3) + i16 : 48 + (i16 & 7));
  const u16* pa = wd + (32 * kp + 4 * g) * D + e;
#pragma unroll
  for (int j = 0; j < 4; ++j) { r[j] = pa[j * D]; r[4 + j] = pa[(16 + j) * D]; }
}
__device__ __forceinline__ void w2_bstore(char* wb, int q, int lane, const unsigned (&r)[8]) {
  const int c = q / 196, rem = q % 196;
  u32x4 v = {r[0] | (r[1] << 16), r[2] | (r[3] << 16), r[4] | (r[5] << 16), r[6] | (r[7] << 16)};
  *(u32x4*)(wb + ((size_t)(c * 42 + 14) * 7 + rem) * 1024 + lane * 16) = v;
}
__device__ __forceinline__ void w2_agather(const u16* wre, const u16* wim, int q, int lane, u32x2& lo, u32x2& hi) {
  const int g = lane >> 4, i16 = lane & 15;
  const int c = q / 98, rem = q % 98, pp = rem / 7, sl = rem % 7;
  const int kp = 2 * pp + (sl < 6 ? sl / 3 : (i16 >> 3));
  const int e = 56 * c + (sl < 6 ? 16 * (sl % 3) + i16 : 48 + (i16 & 7));
  lo = *(const u32x2*)(wre + e * D + 16 * kp + 4 * g);
  hi = *(const u32x2*)(wim + e * D + 16 * kp + 4 * g);
}
__device__ __forceinline__ void w2_astore(char* wb, int q, int lane, u32x2 lo, u32x2 hi) {
  const int c = q / 98, rem = q % 98;
  u32x4 v = {lo[0], lo[1], hi[0] ^ 0x80008000u, hi[1] ^ 0x80008000u};
  *(u32x4*)(wb + ((size_t)(c * 42) * 7 + rem) * 1024 + lane * 16) = v;
}
__device__ __forceinline__ float dot8(u32x4 a, u32x4 b) {
  float s = 0.f;
#pragma unroll
  for (int j = 0; j < 4; ++j) {
    s = __builtin_fmaf(__builtin_bit_cast(float, a[j] << 16), __builtin_bit_cast(float, b[j] << 16), s);
    s = __builtin_fmaf(__builtin_bit_cast(float, a[j] & 0xffff0000u), __builtin_bit_cast(float, b[j] & 0xffff0000u), s);
  }
  return s;
}

__device__ __forceinline__ void side_task(const char* x, const u16* wre, const u16* wim, const u16* wd, const char* gw,
                                          char* ws, int sw, int lane) {
  char* wb = ws + WB_OFF;
  {
    const int row = 16 * sw + (lane >> 2), part = lane & 3;
    u32x4 xv[14], gv[14];
#pragma unroll
    for (int i = 0; i < 14; ++i) {
      xv[i] = *(const u32x4*)(x + row * 896 + (4 * i + part) * 16);
      gv[i] = *(const u32x4*)(gw + (4 * i + part) * 16);
    }
    float z = 0.f;
#pragma unroll
    for (int i = 0; i < 14; ++i) z += dot8(xv[i], gv[i]);
    z += __shfl_xor(z, 1);
    z += __shfl_xor(z, 2);
    if (part == 0) ((float*)(ws + G_OFF))[row] = sigmoidf_(z);
  }
  {
    u32x2 lo[7], hi[7];
#pragma unroll
    for (int i = 0; i < 7; ++i) {
      const int q = sw + 128 * i;
      if (i < 6 || q < 784) w2_agather(wre, wim, q, lane, lo[i], hi[i]);
    }
#pragma unroll
    for (int i = 0; i < 7; ++i) {
      const int q = sw + 128 * i;
      if (i < 6 || q < 784) w2_astore(wb, q, lane, lo[i], hi[i]);
    }
  }
  {
    unsigned r[13][8];
#pragma unroll
    for (int i = 0; i < 13; ++i) {
      const int q = sw + 128 * i;
      if (i < 12 || q < 1568) w2_bgather(wd, q, lane, r[i]);
    }
#pragma unroll
    for (int i = 0; i < 13; ++i) {
      const int q = sw + 128 * i;
      if (i < 12 || q < 1568) w2_bstore(wb, q, lane, r[i]);
    }
  }
}

__global__ __launch_bounds__(512, 2) void k1(const char* __restrict__ x, const char* __restrict__ cr,
                                             const char* __restrict__ ci, const u16* __restrict__ wre,
                                             const u16* __restrict__ wim, const char* __restrict__ wg,
                                             const char* __restrict__ wu, const u16* __restrict__ wd,
                                             const char* __restrict__ gw, char* __restrict__ ws) {
  extern __shared__ __attribute__((aligned(16))) char lds[];
  const unsigned lds0 = (unsigned)(size_t)(LDS3 char*)lds;
  const int lane = threadIdx.x & 63;
  const int w = __builtin_amdgcn_readfirstlane(threadIdx.x >> 6);
  const int xcd = blockIdx.x & 7, idx = blockIdx.x >> 3;
  u64* stp = (u64*)(ws + ST_OFF);
  {
    const int wv = w;
    RSTAMP(8);
    STAMP(0);
  }
  if (idx < 12) {
    const int tl = idx / 3;
    pq_task<8>(x, cr, ci, ws, lds0, 4 * xcd + tl, idx - 3 * tl, w, lane, stp);
  } else if (idx < 16) {
    pq_task<4>(x, cr, ci, ws, lds0, 4 * xcd + idx - 12, 3, w, lane, stp);
  } else if (idx < 30) {
    const int id = 14 * xcd + idx - 16;
    h_task(x, wg, wu, ws, lds0, id >> 3, id & 7, w, lane, stp);
  } else {
    side_task(x, wre, wim, wd, gw, ws, (2 * xcd + idx - 30) * 8 + w, lane);
  }
  {
    const int wv = w;
    RSTAMP(9);
  }
}

constexpr int K2_NW = 6;
constexpr int K2_PP = 42 / K2_NW;
constexpr int K2_RS = 60;
constexpr int K2_LDS = K2_NW * 64 * K2_RS * 4;

struct K2Frag { bf16x8 h[2][4]; bf16x8 w[7]; };

__device__ __forceinline__ void k2_load(K2Frag& f, const char* hp, const char* wp, unsigned vo) {
#pragma unroll
  for (int s = 0; s < 2; ++s)
#pragma unroll
    for (int m = 0; m < 4; ++m) f.h[s][m] = *(const bf16x8*)(hp + s * 4096 + m * 1024 + vo);
#pragma unroll
  for (int n = 0; n < 7; ++n) f.w[n] = *(const bf16x8*)(wp + n * 1024 + vo);
}
__device__ __forceinline__ void k2_mma(f32x4 (&acc)[5][4], const K2Frag& f) {
#pragma unroll
  for (int s = 0; s < 2; ++s) {
#pragma unroll
    for (int n = 0; n < 3; ++n)
#pragma unroll
      for (int m = 0; m < 4; ++m) acc[n][m] = mfma(f.w[3 * s + n], f.h[s][m], acc[n][m]);
#pragma unroll
    for (int m = 0; m < 4; ++m) acc[3 + s][m] = mfma(f.w[6], f.h[s][m], acc[3 + s][m]);
  }
}

__global__ __launch_bounds__(K2_NW * 64) void k2(const char* __restrict__ ws, u16* __restrict__ out, u64* stp) {
  extern __shared__ __attribute__((aligned(16))) char lds[];
  const int lane = threadIdx.x & 63;
  const int w = __builtin_amdgcn_readfirstlane(threadIdx.x >> 6);
  const int wv = w;
  RSTAMP(10);
  STAMP(11);
  const int xcd = blockIdx.x & 7, idx = blockIdx.x >> 3;
  const int tb = 4 * xcd + (idx >> 3), c = idx & 7;
  const char* hp = ws + (size_t)(tb * NK2 + 2 * K2_PP * w) * 4096;
  const char* wp = ws + WB_OFF + (size_t)(c * 42 + K2_PP * w) * 7168;
  const unsigned vo = lane * 16;
  const int g = lane >> 4, i16 = lane & 15;

  f32x4 acc[5][4];
#pragma unroll
  for (int n = 0; n < 5; ++n)
#pragma unroll
    for (int m = 0; m < 4; ++m) acc[n][m] = f32x4{0, 0, 0, 0};
  K2Frag f[2];
  float gt[4];
#pragma unroll
  for (int m = 0; m < 4; ++m) gt[m] = ((const float*)(ws + G_OFF))[64 * tb + 16 * m + i16];
  SCHED0;
  k2_load(f[0], hp, wp, vo);
  SCHED0;
  k2_load(f[1], hp + 8192, wp + 7168, vo);
#pragma unroll
  for (int p = 0; p < K2_PP; ++p) {
    SCHED0;
    k2_mma(acc, f[p & 1]);
    SCHED0;
    if (p + 2 < K2_PP) k2_load(f[p & 1], hp + (p + 2) * 8192, wp + (p + 2) * 7168, vo);
  }
  SCHED0;
  STAMP(12);
  const bool isA = w < K2_NW / 3;
  LDS3 char* red = (LDS3 char*)lds + w * (64 * K2_RS * 4);
#pragma unroll
  for (int m = 0; m < 4; ++m) {
    const float sc = isA ? gt[m] : 1.0f - gt[m];
#pragma unroll
    for (int n = 0; n < 3; ++n)
      *(LDS3 f32x4*)(red + ((16 * m + i16) * K2_RS + 16 * n + 4 * g) * 4) = acc[n][m] * sc;
    f32x4 v;
#pragma unroll
    for (int r = 0; r < 4; ++r) {
      const float own = g < 2 ? acc[3][m][r] : acc[4][m][r];
      v[r] = (own + __shfl_xor(own, 32)) * sc;
    }
    if (g < 2) *(LDS3 f32x4*)(red + ((16 * m + i16) * K2_RS + 48 + 4 * g) * 4) = v;
  }
  __syncthreads();
#pragma unroll
  for (int rnd = 0; rnd < 2; ++rnd) {
    const int it = threadIdx.x + rnd * (K2_NW * 64);
    if (it < 448) {
      const int t = it / 7, j = it - 7 * t;
      LDS3 char* p = (LDS3 char*)lds + (t * K2_RS + 8 * j) * 4;
      f32x4 v0[K2_NW], v1[K2_NW];
#pragma unroll
      for (int ww = 0; ww < K2_NW; ++ww) {
        v0[ww] = *(LDS3 f32x4*)(p + ww * (64 * K2_RS * 4));
        v1[ww] = *(LDS3 f32x4*)(p + ww * (64 * K2_RS * 4) + 16);
      }
      f32x4 s0 = (v0[0] + v0[1]) + (v0[2] + v0[3]) + (v0[4] + v0[5]);
      f32x4 s1 = (v1[0] + v1[1]) + (v1[2] + v1[3]) + (v1[4] + v1[5]);
      u32x4 v = {pk(s0[0], s0[1]), pk(s0[2], s0[3]), pk(s1[0], s1[1]), pk(s1[2], s1[3])};
      *(u32x4*)(out + (size_t)(64 * tb + t) * D + 56 * c + 8 * j) = v;
    }
  }
  STAMP(13);
  RSTAMP(14);
}

}

extern "C" void kernel_launch(void* const* d_in, const int* in_sizes, int n_in, void* d_out, int out_size,
                              void* d_ws, size_t ws_size, hipStream_t stream) {
  (void)in_sizes; (void)n_in; (void)out_size; (void)ws_size;
  (void)hipFuncSetAttribute((const void*)k1, hipFuncAttributeMaxDynamicSharedMemorySize, K1_LDS);
  (void)hipFuncSetAttribute((const void*)k2, hipFuncAttributeMaxDynamicSharedMemorySize, K2_LDS);
  k1<<<256, 512, K1_LDS, stream>>>((const char*)d_in[0], (const char*)d_in[1], (const char*)d_in[2],
                                   (const u16*)d_in[3], (const u16*)d_in[4], (const char*)d_in[6],
                                   (const char*)d_in[7], (const u16*)d_in[8], (const char*)d_in[9], (char*)d_ws);
  k2<<<256, K2_NW * 64, K2_LDS, stream>>>((const char*)d_ws, (u16*)d_out, (u64*)((char*)d_ws + ST_OFF));
}
```

```cpp
#include <hip/hip_runtime.h>
#include <stdint.h>

namespace {

typedef unsigned short u16;
typedef unsigned long long u64;
typedef __attribute__((ext_vector_type(8))) __bf16 bf16x8;
typedef __attribute__((ext_vector_type(4))) __bf16 bf16x4;
typedef __attribute__((ext_vector_type(2))) __bf16 bf16x2;
typedef __attribute__((ext_vector_type(4))) float f32x4;
typedef __attribute__((ext_vector_type(2))) float f32x2;
typedef __attribute__((ext_vector_type(4))) unsigned u32x4;
typedef __attribute__((ext_vector_type(2))) unsigned u32x2;
#define LDS3 __attribute__((address_space(3)))

constexpr int S = 2048, D = 448, F = 1792;
constexpr int NK2 = 84;
constexpr size_t HB_BYTES = (size_t)32 * NK2 * 4096;
constexpr size_t WB_OFF = HB_BYTES;
constexpr size_t WB_BYTES = (size_t)8 * 42 * 7 * 1024;
constexpr size_t G_OFF = WB_OFF + WB_BYTES;
constexpr size_t ST_OFF = (size_t)64 << 20;

#define PQ_KSTART(tb, ds) ((8 * (ds) + ((tb) >> 2)) & 31)
#define PQ_TOUCH 0
#define PQ_TSLEEP 4
#define PQ_T0 5
#define PQ_NPRO 1
#define PQ_TDELAY 0
#define H_PIN 1
#define K2_PIN 1
constexpr int PQ_NS = 5;
constexpr int H_XSLOT = 32768;
constexpr int H_WOFF = 3 * H_XSLOT;
constexpr int H_WSLOT = 16384;
constexpr int K1_LDS = H_WOFF + 4 * H_WSLOT;

#define STAMP(i) do {} while (0)
#define RSTAMP(i) do {} while (0)

template <int IMM>
__device__ __forceinline__ void glds(unsigned voff, const char* sbase, unsigned ldsbase) {
  asm volatile("s_add_u32 m0, %2, %3\n\ts_nop 0\n\tglobal_load_lds_dwordx4 %0, %1"
               :: "v"(voff), "s"(sbase), "s"(ldsbase), "i"(IMM) : "memory", "scc");
}
#define C_MOD ""
template <int IMM>
__device__ __forceinline__ void glds_c(unsigned voff, const char* sbase, unsigned ldsbase) {
  asm volatile("s_add_u32 m0, %2, %3\n\ts_nop 0\n\tglobal_load_lds_dwordx4 %0, %1" C_MOD
               :: "v"(voff), "s"(sbase), "s"(ldsbase), "i"(IMM) : "memory", "scc");
}
#define X_MOD ""
#define H_MOD ""
template <int IMM>
__device__ __forceinline__ void glds_x(unsigned voff, const char* sbase, unsigned ldsbase) {
  asm volatile("s_add_u32 m0, %2, %3\n\ts_nop 0\n\tglobal_load_lds_dwordx4 %0, %1" X_MOD
               :: "v"(voff), "s"(sbase), "s"(ldsbase), "i"(IMM) : "memory", "scc");
}
template <int IMM>
__device__ __forceinline__ void glds_h(unsigned voff, const char* sbase, unsigned ldsbase) {
  asm volatile("s_add_u32 m0, %2, %3\n\ts_nop 0\n\tglobal_load_lds_dwordx4 %0, %1" H_MOD
               :: "v"(voff), "s"(sbase), "s"(ldsbase), "i"(IMM) : "memory", "scc");
}
#define ST_MOD " sc1"
#define OUT_MOD ""
__device__ __forceinline__ void st16(void* p, u32x4 v) {
  asm volatile("global_store_dwordx4 %0, %1, off" ST_MOD "\n\ts_nop 1" :: "v"(p), "v"(v) : "memory");
}
__device__ __forceinline__ void st16o(void* p, u32x4 v) {
  asm volatile("global_store_dwordx4 %0, %1, off" OUT_MOD "\n\ts_nop 1" :: "v"(p), "v"(v) : "memory");
}
__device__ __forceinline__ void st4(void* p, float v) {
  asm volatile("global_store_dword %0, %1, off" ST_MOD :: "v"(p), "v"(v) : "memory");
}
template <int N> __device__ __forceinline__ void wait_vm() { asm volatile("s_waitcnt vmcnt(%0)" :: "i"(N) : "memory"); }
template <int N> __device__ __forceinline__ void wait_vm_bar() {
  asm volatile("s_waitcnt vmcnt(%0)\n\ts_barrier" :: "i"(N) : "memory");
}
__device__ __forceinline__ void wait_lgkm_bar() { asm volatile("s_waitcnt lgkmcnt(0)\n\ts_barrier" ::: "memory"); }
__device__ __forceinline__ void bar() { asm volatile("s_barrier" ::: "memory"); }
#define SCHED0 __builtin_amdgcn_sched_barrier(0)

__device__ __forceinline__ bf16x8 lds_b128(unsigned addr) { return *(LDS3 bf16x8*)(addr); }
template <int HI>
__device__ __forceinline__ bf16x8 lds_tr2(unsigned addr) {
  bf16x4 lo = __builtin_amdgcn_ds_read_tr16_b64_v4bf16((LDS3 bf16x4*)(addr));
  bf16x4 hi = __builtin_amdgcn_ds_read_tr16_b64_v4bf16((LDS3 bf16x4*)(addr + HI));
  return __builtin_shufflevector(lo, hi, 0, 1, 2, 3, 4, 5, 6, 7);
}
__device__ __forceinline__ f32x4 mfma(bf16x8 a, bf16x8 b, f32x4 c) {
  return __builtin_amdgcn_mfma_f32_16x16x32_bf16(a, b, c, 0, 0, 0);
}
__device__ __forceinline__ unsigned pk(float a, float b) {
  f32x2 v = {a, b};
  bf16x2 p = __builtin_convertvector(v, bf16x2);
  return __builtin_bit_cast(unsigned, p);
}
__device__ __forceinline__ float sigmoidf_(float z) {
  return __builtin_amdgcn_rcpf(1.0f + __builtin_amdgcn_exp2f(-1.4426950408889634f * z));
}
__device__ __forceinline__ int perm32(int p) { return 8 * ((p & 15) >> 2) + (p & 3) + 4 * (p >> 4); }

__device__ __forceinline__ float dot8(u32x4 a, u32x4 b) {
  float s = 0.f;
#pragma unroll
  for (int j = 0; j < 4; ++j) {
    s = __builtin_fmaf(__builtin_bit_cast(float, a[j] << 16), __builtin_bit_cast(float, b[j] << 16), s);
    s = __builtin_fmaf(__builtin_bit_cast(float, a[j] & 0xffff0000u), __builtin_bit_cast(float, b[j] & 0xffff0000u), s);
  }
  return s;
}
__device__ __forceinline__ void gate_rows(const char* x, const char* gw, char* ws, int row0, int lane) {
  const int row = row0 + (lane >> 2), part = lane & 3;
  u32x4 xv[14], gv[14];
#pragma unroll
  for (int i = 0; i < 14; ++i) {
    xv[i] = *(const u32x4*)(x + row * 896 + (4 * i + part) * 16);
    gv[i] = *(const u32x4*)(gw + (4 * i + part) * 16);
  }
  float z = 0.f;
#pragma unroll
  for (int i = 0; i < 14; ++i) z += dot8(xv[i], gv[i]);
  z += __shfl_xor(z, 1);
  z += __shfl_xor(z, 2);
  if (part == 0) st4(ws + G_OFF + row * 4, sigmoidf_(z));
}

template <int NJ> struct Pq {
  static constexpr int ROWB = NJ * 32;
  static constexpr int XSLOT = 64 * ROWB;
  static constexpr int XOFF = PQ_NS * 16384;
  static constexpr int NX = NJ / 2;
  static constexpr int PER = 4 + NX;
  static constexpr int NH = NJ / 2;
  __device__ static __forceinline__ int swz(int pos) { return NJ == 8 ? (pos & 7) : ((pos >> 1) & 3); }
};

template <int NJ>
__device__ __forceinline__ void pq_loader(const char* cr, const char* ci, const char* x, const char* gw, char* ws,
                                          unsigned lds0, int tb, int ds, int lw, int lane) {
  using P = Pq<NJ>;
  unsigned vC[4], vX[P::NX];
#pragma unroll
  for (int qq = 0; qq < 4; ++qq) {
    int row = 64 * tb + 32 * (lw & 1) + 8 * qq + (lane >> 3);
    int c = (lane & 7) ^ ((4 * (qq & 1) + (lane >> 4)) & 7);
    vC[qq] = row * 4096 + c * 16;
  }
#pragma unroll
  for (int qq = 0; qq < P::NX; ++qq) {
    constexpr int LPR = P::ROWB / 16;
    int pos = (P::NX * lw + qq) * (64 / LPR) + lane / LPR;
    int c = lane % LPR;
    int s = (pos & 32) + perm32(pos & 31);
    vX[qq] = s * 896 + ds * 256 + (((c >> 1) ^ P::swz(pos)) << 5) + ((c & 1) << 4);
  }
  const char* cb0 = (lw >> 1) ? ci : cr;
  const unsigned ldsC = lds0 + lw * 4096, ldsX = lds0 + P::XOFF + P::NX * lw * 1024;
  int kk = PQ_KSTART(tb, ds);
  asm volatile("s_nop 4" ::: "memory");
  auto issue = [&](unsigned slot) {
    const char* cb = cb0 + kk * 128;
    const char* xb = x + kk * (64 * 896);
    const unsigned lc = ldsC + slot * 16384, lx = ldsX + slot * P::XSLOT;
    glds_c<0>(vC[0], cb, lc);
    glds_c<1024>(vC[1], cb, lc);
    glds_c<2048>(vC[2], cb, lc);
    glds_c<3072>(vC[3], cb, lc);
    glds_x<0>(vX[0], xb, lx);
    glds_x<1024>(vX[1], xb, lx);
    if constexpr (P::NX == 4) {
      glds_x<2048>(vX[2], xb, lx);
      glds_x<3072>(vX[3], xb, lx);
    }
    kk = (kk + 1) & 31;
  };
  issue(0);
  issue(1);
  wait_vm_bar<P::PER>();
  issue(2);
  issue(3);
  issue(4);
  unsigned slot = 0;
  for (int i = 0; i < 32 - PQ_NS; ++i) {
    wait_vm_bar<P::PER*(PQ_NS - 2)>();
    issue(slot);
    slot = (slot == PQ_NS - 1) ? 0 : slot + 1;
  }
  wait_vm_bar<P::PER * 3>();
  wait_vm_bar<P::PER * 2>();
  wait_vm_bar<P::PER * 1>();
  wait_vm_bar<0>();
  if constexpr (NJ == 4) gate_rows(x, gw, ws, 64 * tb + 16 * lw, lane);
}

template <int NH> struct PqFrag { bf16x8 a[NH]; bf16x8 b[4]; };

template <int NH> struct PqAddr { unsigned c[2][2]; unsigned x[2][NH]; };

template <int NJ, int SL, int KK>
__device__ __forceinline__ void pq_read(PqFrag<NJ / 2>& f, const PqAddr<NJ / 2>& a) {
  using P = Pq<NJ>;
  constexpr int RB = P::ROWB, W = SL / 4;
  constexpr int oc = (SL % 4) * 16384, ox = (SL % 4) * P::XSLOT + KK * 32 * RB;
  f.b[0] = lds_b128(a.c[W][KK] + oc);
  f.a[0] = lds_tr2<16 * RB>(a.x[W][0] + ox);
  f.b[1] = lds_b128(a.c[W][KK] + oc + 8192);
  f.b[2] = lds_b128(a.c[W][KK] + oc + 2048);
  f.b[3] = lds_b128(a.c[W][KK] + oc + 8192 + 2048);
#pragma unroll
  for (int j = 1; j < NJ / 2; ++j) f.a[j] = lds_tr2<16 * RB>(a.x[W][j] + ox);
}
template <int NH>
__device__ __forceinline__ void pq_mma(f32x4 (&acc)[NH][4], const PqFrag<NH>& f) {
#pragma unroll
  for (int j = 0; j < NH; ++j)
#pragma unroll
    for (int m = 0; m < 4; ++m) acc[j][m] = mfma(f.a[j], f.b[m], acc[j][m]);
}
template <int NH>
__device__ __forceinline__ void pq_pattern() {
  constexpr int NR = 4 + 2 * NH, NM = 4 * NH;
  constexpr int DBL = (NH == 4) ? 2 : 2;
#pragma unroll
  for (int j = 0; j < DBL; ++j) {
    __builtin_amdgcn_sched_group_barrier(0x100, 2, 0);
    __builtin_amdgcn_sched_group_barrier(0x8, 1, 0);
  }
#pragma unroll
  for (int j = 0; j < NR - 2 * DBL; ++j) {
    __builtin_amdgcn_sched_group_barrier(0x100, 1, 0);
    __builtin_amdgcn_sched_group_barrier(0x8, 1, 0);
  }
  __builtin_amdgcn_sched_group_barrier(0x8, NM - (NR - DBL), 0);
}

template <int NJ, int SL>
__device__ __forceinline__ void pq_step(f32x4 (&acc)[NJ / 2][4], PqFrag<NJ / 2>& f0, PqFrag<NJ / 2>& f1,
                                        const PqAddr<NJ / 2>& a) {
  SCHED0;
  pq_read<NJ, SL, 1>(f1, a);
  pq_mma<NJ / 2>(acc, f0);
  pq_pattern<NJ / 2>();
  SCHED0;
  wait_lgkm_bar();
  SCHED0;
  pq_read<NJ, (SL + 1) % PQ_NS, 0>(f0, a);
  pq_mma<NJ / 2>(acc, f1);
  pq_pattern<NJ / 2>();
}
template <int NJ>
__device__ __forceinline__ void pq_steps5(f32x4 (&acc)[NJ / 2][4], PqFrag<NJ / 2>& f0, PqFrag<NJ / 2>& f1,
                                          const PqAddr<NJ / 2>& a) {
  pq_step<NJ, 0>(acc, f0, f1, a);
  pq_step<NJ, 1>(acc, f0, f1, a);
  pq_step<NJ, 2>(acc, f0, f1, a);
  pq_step<NJ, 3>(acc, f0, f1, a);
  pq_step<NJ, 4>(acc, f0, f1, a);
}

__device__ __forceinline__ void pq_touch(unsigned& sink, const char* cr, const char* ci, int tb, int ds, int cw, int lane,
                                         int j, u64 mask) {
  const int id = 4 * ds + cw;
  const int k0 = PQ_KSTART(tb, ds) + PQ_T0;
  const char* tp = ((cw & 1) ? ci : cr) + ((k0 + 2 * j + (cw >> 1)) & 31) * 128;
  const unsigned vo = (64 * tb + lane) * 4096;
  asm volatile("s_mov_b64 exec, %3\n\tglobal_load_dword %0, %1, %2\n\ts_mov_b64 exec, -1"
               : "+v"(sink) : "v"(vo), "s"(tp), "s"(mask) : "memory");
}

template <int NJ>
__device__ __forceinline__ void pq_consumer(char* hb, const char* cr, const char* ci, unsigned lds0, int tb, int ds,
                                            int cw, int lane, u64* stp) {
  using P = Pq<NJ>;
  constexpr int NH = P::NH;
  const int wv = cw;
  const int g = lane >> 4, i16 = lane & 15;
  const int wm = cw & 1, wn = cw >> 1;
  const int pos = 4 * g + (i16 >> 2);
  PqAddr<NH> a;
#pragma unroll
  for (int W = 0; W < 2; ++W) {
#pragma unroll
    for (int j = 0; j < NH; ++j) {
      a.x[W][j] = lds0 + P::XOFF + W * 4 * P::XSLOT + pos * P::ROWB + (((NH * wn + j) ^ P::swz(pos)) << 5) + 8 * (lane & 3);
      asm volatile("" : "+v"(a.x[W][j]));
    }
#pragma unroll
    for (int k = 0; k < 2; ++k) {
      a.c[W][k] = (lds0 + W * 65536 + (32 * wm + i16) * 128 + ((g ^ (i16 >> 1)) << 4)) ^ (k * 64);
      asm volatile("" : "+v"(a.c[W][k]));
    }
  }
  unsigned sink = 0;
  f32x4 acc[NH][4];
#pragma unroll
  for (int j = 0; j < NH; ++j)
#pragma unroll
    for (int m = 0; m < 4; ++m) acc[j][m] = f32x4{0, 0, 0, 0};
  PqFrag<NH> f0, f1;
  bar();
  STAMP(1);
  pq_read<NJ, 0, 0>(f0, a);
#pragma unroll 1
  for (int it = 0; it < 6; ++it) {
    pq_steps5<NJ>(acc, f0, f1, a);
  }
  pq_step<NJ, 0>(acc, f0, f1, a);
  SCHED0;
  pq_read<NJ, 1, 1>(f1, a);
  pq_mma<NH>(acc, f0);
  pq_pattern<NH>();
  SCHED0;
  pq_mma<NH>(acc, f1);
  wait_vm<0>();
  asm volatile("" :: "v"(sink));
  STAMP(2);
  char* dst = hb + ((size_t)(tb * NK2 + 8 * ds + NH * wn) * 4 + 2 * wm) * 1024 + lane * 16;
#pragma unroll
  for (int j = 0; j < NH; ++j)
#pragma unroll
    for (int mt = 0; mt < 2; ++mt) {
      const f32x4 p = acc[j][2 * mt], q = acc[j][2 * mt + 1];
      u32x4 v = {pk(p[0], p[1]), pk(p[2], p[3]), pk(q[0], q[1]), pk(q[2], q[3])};
      st16(dst + j * 4096 + mt * 1024, v);
    }
}

template <int NJ>
__device__ __forceinline__ void pq_task(const char* x, const char* cr, const char* ci, const char* gw, char* ws,
                                        unsigned lds0, int tb, int ds, int w, int lane, u64* stp) {
  if (w < 4) pq_consumer<NJ>(ws, cr, ci, lds0, tb, ds, w, lane, stp);
  else pq_loader<NJ>(cr, ci, x, gw, ws, lds0, tb, ds, w - 4, lane);
}

struct HAddr { unsigned vW[2]; unsigned vX[4]; const char* w; const char* x; unsigned lW; unsigned lX; };

template <int U, int Q>
__device__ __forceinline__ void h_issue_w(const HAddr& a) {
  glds_h<(U & 3) * H_WSLOT + Q * 1024>(a.vW[Q], a.w + (size_t)U * (32 * 3584), a.lW);
}
template <int J, int Q>
__device__ __forceinline__ void h_issue_x(const HAddr& a) {
  glds_h<(J % 3) * H_XSLOT + Q * 1024>(a.vX[Q], a.x + J * 128, a.lX);
}
template <int U, int I>
__device__ __forceinline__ void h_issue(const HAddr& a) {
  if constexpr (I < 2) {
    if constexpr (U + 3 <= 13) h_issue_w<U + 3, I>(a);
  } else {
    if constexpr (U / 2 + 2 <= 6) h_issue_x<U / 2 + 2, 2 * (U & 1) + I - 2>(a);
  }
}
constexpr int h_window(int U) { return U < 0 ? 6 : (U + 3 <= 13 ? 2 : 0) + (U / 2 + 2 <= 6 ? 2 : 0); }

template <int U, int N>
__device__ __forceinline__ bf16x8 h_read_a(const unsigned (&vw)[4]) {
  return lds_tr2<4096>(vw[N >> 1] + (U & 3) * H_WSLOT + (N & 1) * 8192);
}
template <int U>
__device__ __forceinline__ void h_read_b(bf16x8 (&b)[4], unsigned vx) {
  constexpr int J = U >> 1, KK = U & 1;
  const unsigned base = (vx ^ (KK * 64)) + (J % 3 == 2 ? 2 * H_XSLOT : 0);
#pragma unroll
  for (int m = 0; m < 4; ++m) b[m] = lds_b128(base + (J % 3 == 1 ? H_XSLOT : 0) + m * 2048);
}

template <int G, int U, int N>
__device__ __forceinline__ void h_frag(f32x4 (&acc)[8][4], bf16x8 (&a)[4], bf16x8 (&bc)[4], bf16x8 (&bn)[4],
                                       const HAddr& ad, const unsigned (&vw)[4], unsigned vx) {
  if constexpr (N == 4 && U < 13) {
    SCHED0;
    wait_vm_bar<h_window(U - 1)>();
    SCHED0;
    h_read_b<U + 1>(bn, vx);
  }
  const bf16x8 cur = a[N & 3];
#pragma unroll
  for (int m = 0; m < 4; ++m) acc[N][m] = mfma(cur, bc[m], acc[N][m]);
  if constexpr (N + 4 < 8) a[N & 3] = h_read_a<U, N + 4>(vw);
  else if constexpr (U < 13) a[N & 3] = h_read_a<U + 1, N - 4>(vw);
  if constexpr ((N & 1) == G) {
    constexpr int slot = ((N + 4) & 7) >> 1;
    constexpr int win = (N >= 4) ? U : U - 1;
    if constexpr (win >= 0 && win < 13) h_issue<win, slot>(ad);
  }
  SCHED0;
}
template <int G, int U, int N>
__device__ __forceinline__ void h_frags(f32x4 (&acc)[8][4], bf16x8 (&a)[4], bf16x8 (&bc)[4], bf16x8 (&bn)[4],
                                        const HAddr& ad, const unsigned (&vw)[4], unsigned vx) {
  h_frag<G, U, N>(acc, a, bc, bn, ad, vw, vx);
  if constexpr (N < 7) h_frags<G, U, N + 1>(acc, a, bc, bn, ad, vw, vx);
}
template <int G, int U>
__device__ __forceinline__ void h_steps(f32x4 (&acc)[8][4], bf16x8 (&a)[4], bf16x8 (&b0)[4], bf16x8 (&b1)[4],
                                        const HAddr& ad, const unsigned (&vw)[4], unsigned vx) {
  if constexpr ((U & 1) == 0) h_frags<G, U, 0>(acc, a, b0, b1, ad, vw, vx);
  else h_frags<G, U, 0>(acc, a, b1, b0, ad, vw, vx);
  if constexpr (U < 13) h_steps<G, U + 1>(acc, a, b0, b1, ad, vw, vx);
}

__device__ __forceinline__ void h_task(const char* x, const char* wg, const char* wu, char* ws, unsigned lds0, int fb,
                                       int rb, int w, int lane, u64* stp) {
  const int wv = w;
  const int g = lane >> 4, i16 = lane & 15;
  const int wt = w >> 1, wn = w & 1;
  HAddr ad;
#pragma unroll
  for (int qq = 0; qq < 2; ++qq) {
    int pos = 4 * ((2 * w + qq) & 7) + (lane >> 4);
    int c = lane & 15;
    ad.vW[qq] = perm32(pos) * 3584 + fb * 256 + (((c >> 1) ^ (pos & 7)) << 5) + ((c & 1) << 4);
  }
  ad.w = (w < 4) ? wg : wu;
  ad.lW = lds0 + H_WOFF + w * 2048;
#pragma unroll
  for (int qq = 0; qq < 4; ++qq) {
    int row = 256 * rb + 32 * w + 8 * qq + (lane >> 3);
    int c = (lane & 7) ^ ((4 * (qq & 1) + (lane >> 4)) & 7);
    ad.vX[qq] = row * 896 + c * 16;
  }
  ad.x = x;
  ad.lX = lds0 + w * 4096;
  const int pos = 4 * g + (i16 >> 2);
  unsigned vw[4];
#pragma unroll
  for (int j = 0; j < 4; ++j) vw[j] = lds0 + H_WOFF + pos * 256 + (((4 * wn + j) ^ (pos & 7)) << 5) + 8 * (lane & 3);
  const unsigned vx = lds0 + (64 * wt + i16) * 128 + ((g ^ (i16 >> 1)) << 4);
  STAMP(4);
  asm volatile("s_nop 4" ::: "memory");
  h_issue_w<0, 0>(ad); h_issue_w<0, 1>(ad);
  h_issue_x<0, 0>(ad); h_issue_x<0, 1>(ad); h_issue_x<0, 2>(ad); h_issue_x<0, 3>(ad);
  h_issue_w<1, 0>(ad); h_issue_w<1, 1>(ad);
  STAMP(5);

  f32x4 acc[8][4];
#pragma unroll
  for (int n = 0; n < 8; ++n)
#pragma unroll
    for (int m = 0; m < 4; ++m) acc[n][m] = f32x4{0, 0, 0, 0};
  bf16x8 a[4], b0[4], b1[4];
  wait_vm_bar<2>();
  STAMP(6);
  h_issue_w<2, 0>(ad); h_issue_w<2, 1>(ad);
  h_issue_x<1, 0>(ad); h_issue_x<1, 1>(ad); h_issue_x<1, 2>(ad); h_issue_x<1, 3>(ad);
  STAMP(1);
  SCHED0;
  h_read_b<0>(b0, vx);
  a[0] = h_read_a<0, 0>(vw); a[1] = h_read_a<0, 1>(vw); a[2] = h_read_a<0, 2>(vw); a[3] = h_read_a<0, 3>(vw);
  if (w < 4) h_steps<0, 0>(acc, a, b0, b1, ad, vw, vx);
  else h_steps<1, 0>(acc, a, b0, b1, ad, vw, vx);
  SCHED0;
  STAMP(2);

  char* dst = ws + ((size_t)((4 * rb + wt) * NK2 + 28 + 4 * fb + 2 * wn) * 4) * 1024 + lane * 16;
#pragma unroll
  for (int mt = 0; mt < 4; ++mt)
#pragma unroll
    for (int q = 0; q < 2; ++q) {
      unsigned hp[4];
#pragma unroll
      for (int e = 0; e < 2; ++e) {
        const f32x4 ga = acc[4 * q + 2 * e][mt], up = acc[4 * q + 2 * e + 1][mt];
        float h[4];
#pragma unroll
        for (int r = 0; r < 4; ++r) h[r] = up[r] * sigmoidf_(ga[r]);
        hp[2 * e] = pk(h[0], h[1]);
        hp[2 * e + 1] = pk(h[2], h[3]);
      }
      u32x4 v = {hp[0], hp[1], hp[2], hp[3]};
      st16(dst + q * 4096 + mt * 1024, v);
    }
  STAMP(3);
}

constexpr int SD_AOFF = 448 * 224;
constexpr int SD_ASZ = 25 * 1024;

template <int I>
__device__ __forceinline__ void side_issue_b(const char* wd, unsigned lds0, int j, int w, int lane) {
  const int t = w + 8 * I;
  if (I < 12 || t < 98) {
    const unsigned q = 64 * t + lane, row = q / 14, ch = q - 14 * row;
    glds<I * 8192>((448 * (j >> 2) + row) * 896 + 224 * (j & 3) + 16 * ch, wd, lds0 + w * 1024);
  }
  if constexpr (I < 12) side_issue_b<I + 1>(wd, lds0, j, w, lane);
}
template <int I>
__device__ __forceinline__ void side_issue_a(const char* wre, const char* wim, unsigned lds0, int j, int w, int lane) {
  const int u = w + 8 * I;
  if (I < 6 || u < 50) {
    const int m = u >= 25, t = u - 25 * m;
    const unsigned q0 = 64 * t + lane, q = q0 < 1568 ? q0 : 1567, row = q / 28, ch = q - 28 * row;
    glds<SD_AOFF>((56 * (j >> 1) + row) * 896 + 448 * (j & 1) + 16 * ch, m ? wim : wre, lds0 + m * SD_ASZ + t * 1024);
  }
  if constexpr (I < 6) side_issue_a<I + 1>(wre, wim, lds0, j, w, lane);
}

__device__ __forceinline__ void side_task(const char* wre, const char* wim, const char* wd, char* ws, unsigned lds0, int j,
                                          int w, int lane, u64* stp) {
  const int wv = w;
  char* wb = ws + WB_OFF + lane * 16;
  const int g = lane >> 4, i16 = lane & 15;
  asm volatile("s_nop 4" ::: "memory");
  side_issue_a<0>(wre, wim, lds0, j, w, lane);
  side_issue_b<0>(wd, lds0, j, w, lane);
  RSTAMP(1);
  wait_vm_bar<0>();
  RSTAMP(2);
  u32x2 lo[7], hi[7];
#pragma unroll
  for (int i = 0; i < 7; ++i) {
    const int n = (i < 6 || w == 0) ? w + 8 * i : 0, pp = n / 7, sl = n - 7 * pp;
    const int h = sl < 6 ? sl / 3 : (i16 >> 3);
    const int e = sl < 6 ? 16 * (sl % 3) + i16 : 48 + (i16 & 7);
    const unsigned ad = lds0 + SD_AOFF + e * 448 + pp * 64 + 32 * h + 8 * g;
    lo[i] = *(LDS3 u32x2*)(ad);
    hi[i] = *(LDS3 u32x2*)(ad + SD_ASZ);
  }
  bf16x8 fb[13];
#pragma unroll
  for (int i = 0; i < 13; ++i) {
    const int n = (i < 12 || w < 2) ? w + 8 * i : 0, c = n / 49, rem = n - 49 * c, pp = rem / 7, sl = rem - 7 * pp;
    const int row = 64 * pp + 32 * (sl < 6 ? sl / 3 : ((lane & 3) >> 1)) + 4 * g + (i16 >> 2);
    const int col = 112 * c + (sl < 6 ? 32 * (sl % 3) + 8 * (lane & 3) : 96 + 8 * (lane & 1));
    fb[i] = lds_tr2<16 * 224>(lds0 + row * 224 + col);
  }
#pragma unroll
  for (int i = 0; i < 7; ++i) {
    const int n = w + 8 * i;
    const u32x4 v = {lo[i][0], lo[i][1], hi[i][0] ^ 0x80008000u, hi[i][1] ^ 0x80008000u};
    if (i < 6 || n < 49) st16(wb + (size_t)(((j >> 1) * 42 + 7 * (j & 1)) * 7 + n) * 1024, v);
  }
#pragma unroll
  for (int i = 0; i < 13; ++i) {
    const int n = w + 8 * i, c = n / 49, rem = n - 49 * c;
    if (i < 12 || n < 98)
      st16(wb + (size_t)(((2 * (j & 3) + c) * 42 + 14 + 7 * (j >> 2)) * 7 + rem) * 1024, __builtin_bit_cast(u32x4, fb[i]));
  }
  RSTAMP(3);
  RSTAMP(4);
}

__global__ __launch_bounds__(512, 2) void k1(const char* __restrict__ x, const char* __restrict__ cr,
                                             const char* __restrict__ ci, const char* __restrict__ wre,
                                             const char* __restrict__ wim, const char* __restrict__ wg,
                                             const char* __restrict__ wu, const char* __restrict__ wd,
                                             const char* __restrict__ gw, char* __restrict__ ws) {
  extern __shared__ __attribute__((aligned(16))) char lds[];
  const unsigned lds0 = (unsigned)(size_t)(LDS3 char*)lds;
  const int lane = threadIdx.x & 63;
  const int w = __builtin_amdgcn_readfirstlane(threadIdx.x >> 6);
  const int xcd = blockIdx.x & 7, idx = blockIdx.x >> 3;
  u64* stp = (u64*)(ws + ST_OFF);
  {
    const int wv = w;
    RSTAMP(8);
    STAMP(0);
  }
  if (idx < 12) {
    const int tl = idx / 3;
    pq_task<8>(x, cr, ci, gw, ws, lds0, 4 * xcd + tl, idx - 3 * tl, w, lane, stp);
  } else if (idx < 16) {
    pq_task<4>(x, cr, ci, gw, ws, lds0, 4 * xcd + idx - 12, 3, w, lane, stp);
  } else if (idx < 30) {
    const int id = 14 * xcd + idx - 16;
    h_task(x, wg, wu, ws, lds0, id >> 3, id & 7, w, lane, stp);
  } else {
    side_task(wre, wim, wd, ws, lds0, 2 * xcd + idx - 30, w, lane, stp);
  }
  {
    const int wv = w;
    RSTAMP(9);
  }
}

constexpr int K2_NW = 6;
constexpr int K2_PP = 42 / K2_NW;
constexpr int K2_RS = 60;
constexpr int K2_LDS = K2_NW * 64 * K2_RS * 4;

struct K2Frag { bf16x8 h[2][4]; bf16x8 w[7]; };

__device__ __forceinline__ void k2_load(K2Frag& f, const char* hp, const char* wp, unsigned vo) {
#pragma unroll
  for (int s = 0; s < 2; ++s)
#pragma unroll
    for (int m = 0; m < 4; ++m) f.h[s][m] = *(const bf16x8*)(hp + s * 4096 + m * 1024 + vo);
#pragma unroll
  for (int n = 0; n < 7; ++n) f.w[n] = *(const bf16x8*)(wp + n * 1024 + vo);
}
__device__ __forceinline__ void k2_mma(f32x4 (&acc)[5][4], const K2Frag& f) {
#pragma unroll
  for (int s = 0; s < 2; ++s) {
#pragma unroll
    for (int n = 0; n < 3; ++n)
#pragma unroll
      for (int m = 0; m < 4; ++m) acc[n][m] = mfma(f.w[3 * s + n], f.h[s][m], acc[n][m]);
#pragma unroll
    for (int m = 0; m < 4; ++m) acc[3 + s][m] = mfma(f.w[6], f.h[s][m], acc[3 + s][m]);
  }
}

__global__ __launch_bounds__(K2_NW * 64) void k2(const char* __restrict__ ws, u16* __restrict__ out, u64* stp) {
  extern __shared__ __attribute__((aligned(16))) char lds[];
  const int lane = threadIdx.x & 63;
  const int w = __builtin_amdgcn_readfirstlane(threadIdx.x >> 6);
  const int wv = w;
  RSTAMP(10);
  STAMP(11);
  const int xcd = blockIdx.x & 7, idx = blockIdx.x >> 3;
  const int tb = 4 * xcd + (idx >> 3), c = idx & 7;
  const char* hp = ws + (size_t)(tb * NK2 + 2 * K2_PP * w) * 4096;
  const char* wp = ws + WB_OFF + (size_t)(c * 42 + K2_PP * w) * 7168;
  const unsigned vo = lane * 16;
  const int g = lane >> 4, i16 = lane & 15;

  f32x4 acc[5][4];
#pragma unroll
  for (int n = 0; n < 5; ++n)
#pragma unroll
    for (int m = 0; m < 4; ++m) acc[n][m] = f32x4{0, 0, 0, 0};
  K2Frag f[2];
  float gt[4];
#pragma unroll
  for (int m = 0; m < 4; ++m) gt[m] = ((const float*)(ws + G_OFF))[64 * tb + 16 * m + i16];
  SCHED0;
  k2_load(f[0], hp, wp, vo);
  SCHED0;
  k2_load(f[1], hp + 8192, wp + 7168, vo);
#pragma unroll
  for (int p = 0; p < K2_PP; ++p) {
    SCHED0;
    k2_mma(acc, f[p & 1]);
    SCHED0;
    if (p + 2 < K2_PP) k2_load(f[p & 1], hp + (p + 2) * 8192, wp + (p + 2) * 7168, vo);
  }
  SCHED0;
  STAMP(12);
  const bool isA = w < K2_NW / 3;
  LDS3 char* red = (LDS3 char*)lds + w * (64 * K2_RS * 4);
#pragma unroll
  for (int m = 0; m < 4; ++m) {
    const float sc = isA ? gt[m] : 1.0f - gt[m];
#pragma unroll
    for (int n = 0; n < 3; ++n)
      *(LDS3 f32x4*)(red + ((16 * m + i16) * K2_RS + 16 * n + 4 * g) * 4) = acc[n][m] * sc;
    f32x4 v;
#pragma unroll
    for (int r = 0; r < 4; ++r) {
      const float own = g < 2 ? acc[3][m][r] : acc[4][m][r];
      v[r] = (own + __shfl_xor(own, 32)) * sc;
    }
    if (g < 2) *(LDS3 f32x4*)(red + ((16 * m + i16) * K2_RS + 48 + 4 * g) * 4) = v;
  }
  __syncthreads();
#pragma unroll
  for (int rnd = 0; rnd < 2; ++rnd) {
    const int it = threadIdx.x + rnd * (K2_NW * 64);
    if (it < 448) {
      const int t = it / 7, j = it - 7 * t;
      LDS3 char* p = (LDS3 char*)lds + (t * K2_RS + 8 * j) * 4;
      f32x4 v0[K2_NW], v1[K2_NW];
#pragma unroll
      for (int ww = 0; ww < K2_NW; ++ww) {
        v0[ww] = *(LDS3 f32x4*)(p + ww * (64 * K2_RS * 4));
        v1[ww] = *(LDS3 f32x4*)(p + ww * (64 * K2_RS * 4) + 16);
      }
      f32x4 s0 = (v0[0] + v0[1]) + (v0[2] + v0[3]) + (v0[4] + v0[5]);
      f32x4 s1 = (v1[0] + v1[1]) + (v1[2] + v1[3]) + (v1[4] + v1[5]);
      u32x4 v = {pk(s0[0], s0[1]), pk(s0[2], s0[3]), pk(s1[0], s1[1]), pk(s1[2], s1[3])};
      st16o(out + (size_t)(64 * tb + t) * D + 56 * c + 8 * j, v);
    }
  }
  STAMP(13);
  RSTAMP(14);
}

}

extern "C" void kernel_launch(void* const* d_in, const int* in_sizes, int n_in, void* d_out, int out_size,
                              void* d_ws, size_t ws_size, hipStream_t stream) {
  (void)in_sizes; (void)n_in; (void)out_size; (void)ws_size;
  (void)hipFuncSetAttribute((const void*)k1, hipFuncAttributeMaxDynamicSharedMemorySize, K1_LDS);
  (void)hipFuncSetAttribute((const void*)k2, hipFuncAttributeMaxDynamicSharedMemorySize, K2_LDS);
  k1<<<256, 512, K1_LDS, stream>>>((const char*)d_in[0], (const char*)d_in[1], (const char*)d_in[2],
                                   (const char*)d_in[3], (const char*)d_in[4], (const char*)d_in[6],
                                   (const char*)d_in[7], (const char*)d_in[8], (const char*)d_in[9], (char*)d_ws);
  k2<<<256, K2_NW * 64, K2_LDS, stream>>>((const char*)d_ws, (u16*)d_out, (u64*)((char*)d_ws + ST_OFF));
}
```

```cpp
#include <hip/hip_runtime.h>
#include <stdint.h>

namespace {

typedef unsigned short u16;
typedef unsigned long long u64;
typedef __attribute__((ext_vector_type(8))) __bf16 bf16x8;
typedef __attribute__((ext_vector_type(4))) __bf16 bf16x4;
typedef __attribute__((ext_vector_type(2))) __bf16 bf16x2;
typedef __attribute__((ext_vector_type(4))) float f32x4;
typedef __attribute__((ext_vector_type(2))) float f32x2;
typedef __attribute__((ext_vector_type(4))) unsigned u32x4;
typedef __attribute__((ext_vector_type(2))) unsigned u32x2;
#define LDS3 __attribute__((address_space(3)))

constexpr int S = 2048, D = 448, F = 1792;
constexpr int NK2 = 84;
constexpr size_t HB_BYTES = (size_t)32 * NK2 * 4096;
constexpr size_t WB_OFF = HB_BYTES;
constexpr size_t WB_BYTES = (size_t)8 * 42 * 7 * 1024;
constexpr size_t G_OFF = WB_OFF + WB_BYTES;
constexpr size_t ST_OFF = (size_t)64 << 20;

#define PQ_KSTART(tb, ds) ((8 * (((ds) + (tb)) & 3) + 3 * ((tb) >> 2)) & 31)
#define PQ_KSEQ(s, tb, ds) ((PQ_KSTART(tb, ds) + (s)) & 31)
#define PQ_BLK(s, tb, ds, BK, DL, PHI, BASE) \
  (((BASE) + (((s) + (PHI)) & 31 & ~((BK) - 1)) + (((s) + (PHI) + (DL) * (((ds) + (tb)) & 3)) & ((BK) - 1))) & 31)
#define PQ_DX 0
#define PQ_TOUCH 0
#define PQ_TSLEEP 4
#define PQ_T0 5
#define PQ_NPRO 1
#define PQ_TDELAY 0
#define H_PIN 1
#define K2_PIN 1
constexpr int PQ_NS = 5;
constexpr int H_XSLOT = 32768;
constexpr int H_WOFF = 3 * H_XSLOT;
constexpr int H_WSLOT = 16384;
constexpr int K1_LDS = H_WOFF + 4 * H_WSLOT;

#define STAMP(i) do {} while (0)
#define RSTAMP(i) do {} while (0)

template <int IMM>
__device__ __forceinline__ void glds(unsigned voff, const char* sbase, unsigned ldsbase) {
  asm volatile("s_add_u32 m0, %2, %3\n\ts_nop 0\n\tglobal_load_lds_dwordx4 %0, %1"
               :: "v"(voff), "s"(sbase), "s"(ldsbase), "i"(IMM) : "memory", "scc");
}
#define C_MOD ""
template <int IMM>
__device__ __forceinline__ void glds_c(unsigned voff, const char* sbase, unsigned ldsbase) {
  asm volatile("s_add_u32 m0, %2, %3\n\ts_nop 0\n\tglobal_load_lds_dwordx4 %0, %1" C_MOD
               :: "v"(voff), "s"(sbase), "s"(ldsbase), "i"(IMM) : "memory", "scc");
}
#define X_MOD ""
#define H_MOD ""
template <int IMM>
__device__ __forceinline__ void glds_x(unsigned voff, const char* sbase, unsigned ldsbase) {
  asm volatile("s_add_u32 m0, %2, %3\n\ts_nop 0\n\tglobal_load_lds_dwordx4 %0, %1" X_MOD
               :: "v"(voff), "s"(sbase), "s"(ldsbase), "i"(IMM) : "memory", "scc");
}
template <int IMM>
__device__ __forceinline__ void glds_h(unsigned voff, const char* sbase, unsigned ldsbase) {
  asm volatile("s_add_u32 m0, %2, %3\n\ts_nop 0\n\tglobal_load_lds_dwordx4 %0, %1" H_MOD
               :: "v"(voff), "s"(sbase), "s"(ldsbase), "i"(IMM) : "memory", "scc");
}
#define ST_MOD " sc1"
#define OUT_MOD ""
__device__ __forceinline__ void st16(void* p, u32x4 v) {
  asm volatile("global_store_dwordx4 %0, %1, off" ST_MOD "\n\ts_nop 1" :: "v"(p), "v"(v) : "memory");
}
__device__ __forceinline__ void st16o(void* p, u32x4 v) {
  asm volatile("global_store_dwordx4 %0, %1, off" OUT_MOD "\n\ts_nop 1" :: "v"(p), "v"(v) : "memory");
}
__device__ __forceinline__ void st4(void* p, float v) {
  asm volatile("global_store_dword %0, %1, off" ST_MOD :: "v"(p), "v"(v) : "memory");
}
template <int N> __device__ __forceinline__ void wait_vm() { asm volatile("s_waitcnt vmcnt(%0)" :: "i"(N) : "memory"); }
template <int N> __device__ __forceinline__ void wait_vm_bar() {
  asm volatile("s_waitcnt vmcnt(%0)\n\ts_barrier" :: "i"(N) : "memory");
}
__device__ __forceinline__ void wait_lgkm_bar() { asm volatile("s_waitcnt lgkmcnt(0)\n\ts_barrier" ::: "memory"); }
__device__ __forceinline__ void bar() { asm volatile("s_barrier" ::: "memory"); }
#define SCHED0 __builtin_amdgcn_sched_barrier(0)

__device__ __forceinline__ bf16x8 lds_b128(unsigned addr) { return *(LDS3 bf16x8*)(addr); }
template <int HI>
__device__ __forceinline__ bf16x8 lds_tr2(unsigned addr) {
  bf16x4 lo = __builtin_amdgcn_ds_read_tr16_b64_v4bf16((LDS3 bf16x4*)(addr));
  bf16x4 hi = __builtin_amdgcn_ds_read_tr16_b64_v4bf16((LDS3 bf16x4*)(addr + HI));
  return __builtin_shufflevector(lo, hi, 0, 1, 2, 3, 4, 5, 6, 7);
}
__device__ __forceinline__ f32x4 mfma(bf16x8 a, bf16x8 b, f32x4 c) {
  return __builtin_amdgcn_mfma_f32_16x16x32_bf16(a, b, c, 0, 0, 0);
}
__device__ __forceinline__ unsigned pk(float a, float b) {
  f32x2 v = {a, b};
  bf16x2 p = __builtin_convertvector(v, bf16x2);
  return __builtin_bit_cast(unsigned, p);
}
__device__ __forceinline__ float sigmoidf_(float z) {
  return __builtin_amdgcn_rcpf(1.0f + __builtin_amdgcn_exp2f(-1.4426950408889634f * z));
}
__device__ __forceinline__ int perm32(int p) { return 8 * ((p & 15) >> 2) + (p & 3) + 4 * (p >> 4); }

__device__ __forceinline__ float dot8(u32x4 a, u32x4 b) {
  float s = 0.f;
#pragma unroll
  for (int j = 0; j < 4; ++j) {
    s = __builtin_fmaf(__builtin_bit_cast(float, a[j] << 16), __builtin_bit_cast(float, b[j] << 16), s);
    s = __builtin_fmaf(__builtin_bit_cast(float, a[j] & 0xffff0000u), __builtin_bit_cast(float, b[j] & 0xffff0000u), s);
  }
  return s;
}
__device__ __forceinline__ void gate_rows(const char* x, const char* gw, char* ws, int row0, int lane) {
  const int row = row0 + (lane >> 2), part = lane & 3;
  u32x4 xv[14], gv[14];
#pragma unroll
  for (int i = 0; i < 14; ++i) {
    xv[i] = *(const u32x4*)(x + row * 896 + (4 * i + part) * 16);
    gv[i] = *(const u32x4*)(gw + (4 * i + part) * 16);
  }
  float z = 0.f;
#pragma unroll
  for (int i = 0; i < 14; ++i) z += dot8(xv[i], gv[i]);
  z += __shfl_xor(z, 1);
  z += __shfl_xor(z, 2);
  if (part == 0) st4(ws + G_OFF + row * 4, sigmoidf_(z));
}

template <int NJ> struct Pq {
  static constexpr int ROWB = NJ * 32;
  static constexpr int XSLOT = 64 * ROWB;
  static constexpr int XOFF = PQ_NS * 16384;
  static constexpr int NX = NJ / 2;
  static constexpr int PER = 4 + NX;
  static constexpr int NH = NJ / 2;
  __device__ static __forceinline__ int swz(int pos) { return NJ == 8 ? (pos & 7) : ((pos >> 1) & 3); }
};

template <int NJ>
__device__ __forceinline__ void pq_loader(const char* cr, const char* ci, const char* x, const char* gw, char* ws,
                                          unsigned lds0, int tb, int ds, int lw, int lane) {
  using P = Pq<NJ>;
  unsigned vC[4], vX[P::NX];
#pragma unroll
  for (int qq = 0; qq < 4; ++qq) {
    int row = 64 * tb + 32 * (lw & 1) + 8 * qq + (lane >> 3);
    int c = (lane & 7) ^ ((4 * (qq & 1) + (lane >> 4)) & 7);
    vC[qq] = row * 4096 + c * 16;
  }
#pragma unroll
  for (int qq = 0; qq < P::NX; ++qq) {
    constexpr int LPR = P::ROWB / 16;
    int pos = (P::NX * lw + qq) * (64 / LPR) + lane / LPR;
    int c = lane % LPR;
    int s = (pos & 32) + perm32(pos & 31);
    vX[qq] = s * 896 + ds * 256 + (((c >> 1) ^ P::swz(pos)) << 5) + ((c & 1) << 4);
  }
  const char* cb0 = (lw >> 1) ? ci : cr;
  const unsigned ldsC = lds0 + lw * 4096, ldsX = lds0 + P::XOFF + P::NX * lw * 1024;
  int st = 0;
  asm volatile("s_nop 4" ::: "memory");
  auto issue = [&](unsigned slot) {
    const int kk = PQ_KSEQ(st, tb, ds);
    const char* cb = cb0 + kk * 128;
    const char* xb = x + kk * (64 * 896);
    const unsigned lc = ldsC + slot * 16384, lx = ldsX + slot * P::XSLOT;
    glds_c<0>(vC[0], cb, lc);
    glds_c<1024>(vC[1], cb, lc);
    glds_c<2048>(vC[2], cb, lc);
    glds_c<3072>(vC[3], cb, lc);
    glds_x<0>(vX[0], xb, lx);
    glds_x<1024>(vX[1], xb, lx);
    if constexpr (P::NX == 4) {
      glds_x<2048>(vX[2], xb, lx);
      glds_x<3072>(vX[3], xb, lx);
    }
    ++st;
  };
  issue(0);
  issue(1);
  wait_vm_bar<P::PER>();
  issue(2);
  issue(3);
  issue(4);
  unsigned slot = 0;
  for (int i = 0; i < 32 - PQ_NS; ++i) {
    wait_vm_bar<P::PER*(PQ_NS - 2 - PQ_DX)>();
    issue(slot);
    slot = (slot == PQ_NS - 1) ? 0 : slot + 1;
  }
  wait_vm_bar<P::PER * 3>();
  wait_vm_bar<P::PER * 2>();
  wait_vm_bar<P::PER * 1>();
  wait_vm_bar<0>();
  if constexpr (NJ == 4) gate_rows(x, gw, ws, 64 * tb + 16 * lw, lane);
}

template <int NH> struct PqFrag { bf16x8 a[NH]; bf16x8 b[4]; };

template <int NH> struct PqAddr { unsigned c[2][2]; unsigned x[2][NH]; };

template <int NJ, int SL, int KK>
__device__ __forceinline__ void pq_read(PqFrag<NJ / 2>& f, const PqAddr<NJ / 2>& a) {
  using P = Pq<NJ>;
  constexpr int RB = P::ROWB, W = SL / 4;
  constexpr int oc = (SL % 4) * 16384, ox = (SL % 4) * P::XSLOT + KK * 32 * RB;
  f.b[0] = lds_b128(a.c[W][KK] + oc);
  f.a[0] = lds_tr2<16 * RB>(a.x[W][0] + ox);
  f.b[1] = lds_b128(a.c[W][KK] + oc + 8192);
  f.b[2] = lds_b128(a.c[W][KK] + oc + 2048);
  f.b[3] = lds_b128(a.c[W][KK] + oc + 8192 + 2048);
#pragma unroll
  for (int j = 1; j < NJ / 2; ++j) f.a[j] = lds_tr2<16 * RB>(a.x[W][j] + ox);
}
template <int NH>
__device__ __forceinline__ void pq_mma(f32x4 (&acc)[NH][4], const PqFrag<NH>& f) {
#pragma unroll
  for (int j = 0; j < NH; ++j)
#pragma unroll
    for (int m = 0; m < 4; ++m) acc[j][m] = mfma(f.a[j], f.b[m], acc[j][m]);
}
template <int NH>
__device__ __forceinline__ void pq_pattern() {
  constexpr int NR = 4 + 2 * NH, NM = 4 * NH;
  constexpr int DBL = (NH == 4) ? 2 : 2;
#pragma unroll
  for (int j = 0; j < DBL; ++j) {
    __builtin_amdgcn_sched_group_barrier(0x100, 2, 0);
    __builtin_amdgcn_sched_group_barrier(0x8, 1, 0);
  }
#pragma unroll
  for (int j = 0; j < NR - 2 * DBL; ++j) {
    __builtin_amdgcn_sched_group_barrier(0x100, 1, 0);
    __builtin_amdgcn_sched_group_barrier(0x8, 1, 0);
  }
  __builtin_amdgcn_sched_group_barrier(0x8, NM - (NR - DBL), 0);
}

template <int NJ, int SL>
__device__ __forceinline__ void pq_step(f32x4 (&acc)[NJ / 2][4], PqFrag<NJ / 2>& f0, PqFrag<NJ / 2>& f1,
                                        const PqAddr<NJ / 2>& a) {
  SCHED0;
  pq_read<NJ, SL, 1>(f1, a);
  pq_mma<NJ / 2>(acc, f0);
  pq_pattern<NJ / 2>();
  SCHED0;
  wait_lgkm_bar();
  SCHED0;
  pq_read<NJ, (SL + 1) % PQ_NS, 0>(f0, a);
  pq_mma<NJ / 2>(acc, f1);
  pq_pattern<NJ / 2>();
}
template <int NJ>
__device__ __forceinline__ void pq_steps5(f32x4 (&acc)[NJ / 2][4], PqFrag<NJ / 2>& f0, PqFrag<NJ / 2>& f1,
                                          const PqAddr<NJ / 2>& a) {
  pq_step<NJ, 0>(acc, f0, f1, a);
  pq_step<NJ, 1>(acc, f0, f1, a);
  pq_step<NJ, 2>(acc, f0, f1, a);
  pq_step<NJ, 3>(acc, f0, f1, a);
  pq_step<NJ, 4>(acc, f0, f1, a);
}

__device__ __forceinline__ void pq_touch(unsigned& sink, const char* cr, const char* ci, int tb, int ds, int cw, int lane,
                                         int j, u64 mask) {
  const int id = 4 * ds + cw;
  const int k0 = PQ_KSTART(tb, ds) + PQ_T0;
  const char* tp = ((cw & 1) ? ci : cr) + ((k0 + 2 * j + (cw >> 1)) & 31) * 128;
  const unsigned vo = (64 * tb + lane) * 4096;
  asm volatile("s_mov_b64 exec, %3\n\tglobal_load_dword %0, %1, %2\n\ts_mov_b64 exec, -1"
               : "+v"(sink) : "v"(vo), "s"(tp), "s"(mask) : "memory");
}

template <int NJ>
__device__ __forceinline__ void pq_consumer(char* hb, const char* cr, const char* ci, unsigned lds0, int tb, int ds,
                                            int cw, int lane, u64* stp) {
  using P = Pq<NJ>;
  constexpr int NH = P::NH;
  const int wv = cw;
  const int g = lane >> 4, i16 = lane & 15;
  const int wm = cw & 1, wn = cw >> 1;
  const int pos = 4 * g + (i16 >> 2);
  PqAddr<NH> a;
#pragma unroll
  for (int W = 0; W < 2; ++W) {
#pragma unroll
    for (int j = 0; j < NH; ++j) {
      a.x[W][j] = lds0 + P::XOFF + W * 4 * P::XSLOT + pos * P::ROWB + (((NH * wn + j) ^ P::swz(pos)) << 5) + 8 * (lane & 3);
      asm volatile("" : "+v"(a.x[W][j]));
    }
#pragma unroll
    for (int k = 0; k < 2; ++k) {
      a.c[W][k] = (lds0 + W * 65536 + (32 * wm + i16) * 128 + ((g ^ (i16 >> 1)) << 4)) ^ (k * 64);
      asm volatile("" : "+v"(a.c[W][k]));
    }
  }
  unsigned sink = 0;
  f32x4 acc[NH][4];
#pragma unroll
  for (int j = 0; j < NH; ++j)
#pragma unroll
    for (int m = 0; m < 4; ++m) acc[j][m] = f32x4{0, 0, 0, 0};
  PqFrag<NH> f0, f1;
  bar();
  STAMP(1);
  pq_read<NJ, 0, 0>(f0, a);
#pragma unroll 1
  for (int it = 0; it < 6; ++it) {
    pq_steps5<NJ>(acc, f0, f1, a);
  }
  pq_step<NJ, 0>(acc, f0, f1, a);
  SCHED0;
  pq_read<NJ, 1, 1>(f1, a);
  pq_mma<NH>(acc, f0);
  pq_pattern<NH>();
  SCHED0;
  pq_mma<NH>(acc, f1);
  wait_vm<0>();
  asm volatile("" :: "v"(sink));
  STAMP(2);
  char* dst = hb + ((size_t)(tb * NK2 + 8 * ds + NH * wn) * 4 + 2 * wm) * 1024 + lane * 16;
#pragma unroll
  for (int j = 0; j < NH; ++j)
#pragma unroll
    for (int mt = 0; mt < 2; ++mt) {
      const f32x4 p = acc[j][2 * mt], q = acc[j][2 * mt + 1];
      u32x4 v = {pk(p[0], p[1]), pk(p[2], p[3]), pk(q[0], q[1]), pk(q[2], q[3])};
      st16(dst + j * 4096 + mt * 1024, v);
    }
}

template <int NJ>
__device__ __forceinline__ void pq_task(const char* x, const char* cr, const char* ci, const char* gw, char* ws,
                                        unsigned lds0, int tb, int ds, int w, int lane, u64* stp) {
  if (w < 4) pq_consumer<NJ>(ws, cr, ci, lds0, tb, ds, w, lane, stp);
  else pq_loader<NJ>(cr, ci, x, gw, ws, lds0, tb, ds, w - 4, lane);
}

struct HAddr { unsigned vW[2]; unsigned vX[4]; const char* w; const char* x; unsigned lW; unsigned lX; };

template <int U, int Q>
__device__ __forceinline__ void h_issue_w(const HAddr& a) {
  glds_h<(U & 3) * H_WSLOT + Q * 1024>(a.vW[Q], a.w + (size_t)U * (32 * 3584), a.lW);
}
template <int J, int Q>
__device__ __forceinline__ void h_issue_x(const HAddr& a) {
  glds_h<(J % 3) * H_XSLOT + Q * 1024>(a.vX[Q], a.x + J * 128, a.lX);
}
template <int U, int I>
__device__ __forceinline__ void h_issue(const HAddr& a) {
  if constexpr (I < 2) {
    if constexpr (U + 3 <= 13) h_issue_w<U + 3, I>(a);
  } else {
    if constexpr (U / 2 + 2 <= 6) h_issue_x<U / 2 + 2, 2 * (U & 1) + I - 2>(a);
  }
}
constexpr int h_window(int U) { return U < 0 ? 6 : (U + 3 <= 13 ? 2 : 0) + (U / 2 + 2 <= 6 ? 2 : 0); }

template <int U, int N>
__device__ __forceinline__ bf16x8 h_read_a(const unsigned (&vw)[4]) {
  return lds_tr2<4096>(vw[N >> 1] + (U & 3) * H_WSLOT + (N & 1) * 8192);
}
template <int U>
__device__ __forceinline__ void h_read_b(bf16x8 (&b)[4], unsigned vx) {
  constexpr int J = U >> 1, KK = U & 1;
  const unsigned base = (vx ^ (KK * 64)) + (J % 3 == 2 ? 2 * H_XSLOT : 0);
#pragma unroll
  for (int m = 0; m < 4; ++m) b[m] = lds_b128(base + (J % 3 == 1 ? H_XSLOT : 0) + m * 2048);
}

template <int G, int U, int N>
__device__ __forceinline__ void h_frag(f32x4 (&acc)[8][4], bf16x8 (&a)[4], bf16x8 (&bc)[4], bf16x8 (&bn)[4],
                                       const HAddr& ad, const unsigned (&vw)[4], unsigned vx) {
  if constexpr (N == 4 && U < 13) {
    SCHED0;
    wait_vm_bar<h_window(U - 1)>();
    SCHED0;
    h_read_b<U + 1>(bn, vx);
  }
  const bf16x8 cur = a[N & 3];
#pragma unroll
  for (int m = 0; m < 4; ++m) acc[N][m] = mfma(cur, bc[m], acc[N][m]);
  if constexpr (N + 4 < 8) a[N & 3] = h_read_a<U, N + 4>(vw);
  else if constexpr (U < 13) a[N & 3] = h_read_a<U + 1, N - 4>(vw);
  if constexpr ((N & 1) == G) {
    constexpr int slot = ((N + 4) & 7) >> 1;
    constexpr int win = (N >= 4) ? U : U - 1;
    if constexpr (win >= 0 && win < 13) h_issue<win, slot>(ad);
  }
  SCHED0;
}
template <int G, int U, int N>
__device__ __forceinline__ void h_frags(f32x4 (&acc)[8][4], bf16x8 (&a)[4], bf16x8 (&bc)[4], bf16x8 (&bn)[4],
                                        const HAddr& ad, const unsigned (&vw)[4], unsigned vx) {
  h_frag<G, U, N>(acc, a, bc, bn, ad, vw, vx);
  if constexpr (N < 7) h_frags<G, U, N + 1>(acc, a, bc, bn, ad, vw, vx);
}
template <int G, int U>
__device__ __forceinline__ void h_steps(f32x4 (&acc)[8][4], bf16x8 (&a)[4], bf16x8 (&b0)[4], bf16x8 (&b1)[4],
                                        const HAddr& ad, const unsigned (&vw)[4], unsigned vx) {
  if constexpr ((U & 1) == 0) h_frags<G, U, 0>(acc, a, b0, b1, ad, vw, vx);
  else h_frags<G, U, 0>(acc, a, b1, b0, ad, vw, vx);
  if constexpr (U < 13) h_steps<G, U + 1>(acc, a, b0, b1, ad, vw, vx);
}

__device__ __forceinline__ void h_task(const char* x, const char* wg, const char* wu, char* ws, unsigned lds0, int fb,
                                       int rb, int w, int lane, u64* stp) {
  const int wv = w;
  const int g = lane >> 4, i16 = lane & 15;
  const int wt = w >> 1, wn = w & 1;
  HAddr ad;
#pragma unroll
  for (int qq = 0; qq < 2; ++qq) {
    int pos = 4 * ((2 * w + qq) & 7) + (lane >> 4);
    int c = lane & 15;
    ad.vW[qq] = perm32(pos) * 3584 + fb * 256 + (((c >> 1) ^ (pos & 7)) << 5) + ((c & 1) << 4);
  }
  ad.w = (w < 4) ? wg : wu;
  ad.lW = lds0 + H_WOFF + w * 2048;
#pragma unroll
  for (int qq = 0; qq < 4; ++qq) {
    int row = 256 * rb + 32 * w + 8 * qq + (lane >> 3);
    int c = (lane & 7) ^ ((4 * (qq & 1) + (lane >> 4)) & 7);
    ad.vX[qq] = row * 896 + c * 16;
  }
  ad.x = x;
  ad.lX = lds0 + w * 4096;
  const int pos = 4 * g + (i16 >> 2);
  unsigned vw[4];
#pragma unroll
  for (int j = 0; j < 4; ++j) vw[j] = lds0 + H_WOFF + pos * 256 + (((4 * wn + j) ^ (pos & 7)) << 5) + 8 * (lane & 3);
  const unsigned vx = lds0 + (64 * wt + i16) * 128 + ((g ^ (i16 >> 1)) << 4);
  STAMP(4);
  asm volatile("s_nop 4" ::: "memory");
  h_issue_w<0, 0>(ad); h_issue_w<0, 1>(ad);
  h_issue_x<0, 0>(ad); h_issue_x<0, 1>(ad); h_issue_x<0, 2>(ad); h_issue_x<0, 3>(ad);
  h_issue_w<1, 0>(ad); h_issue_w<1, 1>(ad);
  STAMP(5);

  f32x4 acc[8][4];
#pragma unroll
  for (int n = 0; n < 8; ++n)
#pragma unroll
    for (int m = 0; m < 4; ++m) acc[n][m] = f32x4{0, 0, 0, 0};
  bf16x8 a[4], b0[4], b1[4];
  wait_vm_bar<2>();
  STAMP(6);
  h_issue_w<2, 0>(ad); h_issue_w<2, 1>(ad);
  h_issue_x<1, 0>(ad); h_issue_x<1, 1>(ad); h_issue_x<1, 2>(ad); h_issue_x<1, 3>(ad);
  STAMP(1);
  SCHED0;
  h_read_b<0>(b0, vx);
  a[0] = h_read_a<0, 0>(vw); a[1] = h_read_a<0, 1>(vw); a[2] = h_read_a<0, 2>(vw); a[3] = h_read_a<0, 3>(vw);
  if (w < 4) h_steps<0, 0>(acc, a, b0, b1, ad, vw, vx);
  else h_steps<1, 0>(acc, a, b0, b1, ad, vw, vx);
  SCHED0;
  STAMP(2);

  char* dst = ws + ((size_t)((4 * rb + wt) * NK2 + 28 + 4 * fb + 2 * wn) * 4) * 1024 + lane * 16;
#pragma unroll
  for (int mt = 0; mt < 4; ++mt)
#pragma unroll
    for (int q = 0; q < 2; ++q) {
      unsigned hp[4];
#pragma unroll
      for (int e = 0; e < 2; ++e) {
        const f32x4 ga = acc[4 * q + 2 * e][mt], up = acc[4 * q + 2 * e + 1][mt];
        float h[4];
#pragma unroll
        for (int r = 0; r < 4; ++r) h[r] = up[r] * sigmoidf_(ga[r]);
        hp[2 * e] = pk(h[0], h[1]);
        hp[2 * e + 1] = pk(h[2], h[3]);
      }
      u32x4 v = {hp[0], hp[1], hp[2], hp[3]};
      st16(dst + q * 4096 + mt * 1024, v);
    }
  STAMP(3);
}

constexpr int SD_AOFF = 448 * 224;
constexpr int SD_ASZ = 25 * 1024;

template <int I>
__device__ __forceinline__ void side_issue_b(const char* wd, unsigned lds0, int j, int w, int lane) {
  const int t = w + 8 * I;
  if (I < 12 || t < 98) {
    const unsigned q = 64 * t + lane, row = q / 14, ch = q - 14 * row;
    glds<I * 8192>((448 * (j >> 2) + row) * 896 + 224 * (j & 3) + 16 * ch, wd, lds0 + w * 1024);
  }
  if constexpr (I < 12) side_issue_b<I + 1>(wd, lds0, j, w, lane);
}
template <int I>
__device__ __forceinline__ void side_issue_a(const char* wre, const char* wim, unsigned lds0, int j, int w, int lane) {
  const int u = w + 8 * I;
  if (I < 6 || u < 50) {
    const int m = u >= 25, t = u - 25 * m;
    const unsigned q0 = 64 * t + lane, q = q0 < 1568 ? q0 : 1567, row = q / 28, ch = q - 28 * row;
    glds<SD_AOFF>((56 * (j >> 1) + row) * 896 + 448 * (j & 1) + 16 * ch, m ? wim : wre, lds0 + m * SD_ASZ + t * 1024);
  }
  if constexpr (I < 6) side_issue_a<I + 1>(wre, wim, lds0, j, w, lane);
}

__device__ __forceinline__ void side_task(const char* wre, const char* wim, const char* wd, char* ws, unsigned lds0, int j,
                                          int w, int lane, u64* stp) {
  const int wv = w;
  char* wb = ws + WB_OFF + lane * 16;
  const int g = lane >> 4, i16 = lane & 15;
  asm volatile("s_nop 4" ::: "memory");
  side_issue_a<0>(wre, wim, lds0, j, w, lane);
  side_issue_b<0>(wd, lds0, j, w, lane);
  RSTAMP(1);
  wait_vm_bar<0>();
  RSTAMP(2);
  u32x2 lo[7], hi[7];
#pragma unroll
  for (int i = 0; i < 7; ++i) {
    const int n = (i < 6 || w == 0) ? w + 8 * i : 0, pp = n / 7, sl = n - 7 * pp;
    const int h = sl < 6 ? sl / 3 : (i16 >> 3);
    const int e = sl < 6 ? 16 * (sl % 3) + i16 : 48 + (i16 & 7);
    const unsigned ad = lds0 + SD_AOFF + e * 448 + pp * 64 + 32 * h + 8 * g;
    lo[i] = *(LDS3 u32x2*)(ad);
    hi[i] = *(LDS3 u32x2*)(ad + SD_ASZ);
  }
  bf16x8 fb[13];
#pragma unroll
  for (int i = 0; i < 13; ++i) {
    const int n = (i < 12 || w < 2) ? w + 8 * i : 0, c = n / 49, rem = n - 49 * c, pp = rem / 7, sl = rem - 7 * pp;
    const int row = 64 * pp + 32 * (sl < 6 ? sl / 3 : ((lane & 3) >> 1)) + 4 * g + (i16 >> 2);
    const int col = 112 * c + (sl < 6 ? 32 * (sl % 3) + 8 * (lane & 3) : 96 + 8 * (lane & 1));
    fb[i] = lds_tr2<16 * 224>(lds0 + row * 224 + col);
  }
#pragma unroll
  for (int i = 0; i < 7; ++i) {
    const int n = w + 8 * i;
    const u32x4 v = {lo[i][0], lo[i][1], hi[i][0] ^ 0x80008000u, hi[i][1] ^ 0x80008000u};
    if (i < 6 || n < 49) st16(wb + (size_t)(((j >> 1) * 42 + 7 * (j & 1)) * 7 + n) * 1024, v);
  }
#pragma unroll
  for (int i = 0; i < 13; ++i) {
    const int n = w + 8 * i, c = n / 49, rem = n - 49 * c;
    if (i < 12 || n < 98)
      st16(wb + (size_t)(((2 * (j & 3) + c) * 42 + 14 + 7 * (j >> 2)) * 7 + rem) * 1024, __builtin_bit_cast(u32x4, fb[i]));
  }
  RSTAMP(3);
  RSTAMP(4);
}

__global__ __launch_bounds__(512, 2) void k1(const char* __restrict__ x, const char* __restrict__ cr,
                                             const char* __restrict__ ci, const char* __restrict__ wre,
                                             const char* __restrict__ wim, const char* __restrict__ wg,
                                             const char* __restrict__ wu, const char* __restrict__ wd,
                                             const char* __restrict__ gw, char* __restrict__ ws) {
  extern __shared__ __attribute__((aligned(16))) char lds[];
  const unsigned lds0 = (unsigned)(size_t)(LDS3 char*)lds;
  const int lane = threadIdx.x & 63;
  const int w = __builtin_amdgcn_readfirstlane(threadIdx.x >> 6);
  const int xcd = blockIdx.x & 7, idx = blockIdx.x >> 3;
  u64* stp = (u64*)(ws + ST_OFF);
  {
    const int wv = w;
    RSTAMP(8);
    STAMP(0);
  }
  if (idx < 12) {
    const int tl = idx / 3;
    pq_task<8>(x, cr, ci, gw, ws, lds0, 4 * xcd + tl, idx - 3 * tl, w, lane, stp);
  } else if (idx < 16) {
    pq_task<4>(x, cr, ci, gw, ws, lds0, 4 * xcd + idx - 12, 3, w, lane, stp);
  } else if (idx < 30) {
    const int id = 14 * xcd + idx - 16;
    h_task(x, wg, wu, ws, lds0, id >> 3, id & 7, w, lane, stp);
  } else {
    side_task(wre, wim, wd, ws, lds0, 2 * xcd + idx - 30, w, lane, stp);
  }
  {
    const int wv = w;
    RSTAMP(9);
  }
}

constexpr int K2_NW = 8;
#define K2_FIRST 0x25090400201a140eull
#define K2_COUNT 0x55545666u
#define K2_CA 6
#define K2_CB 5
#define K2_CC 4
#define K2_ILV 1
constexpr int K2_TILE = 64 * 256;
constexpr int K2_LDS = K2_NW * K2_TILE;

struct K2Frag { bf16x8 h[2][4]; bf16x8 w[7]; };

__device__ __forceinline__ void k2_load(K2Frag& f, const char* hp, const char* wp, unsigned vo) {
#pragma unroll
  for (int s = 0; s < 2; ++s)
#pragma unroll
    for (int m = 0; m < 4; ++m) f.h[s][m] = *(const bf16x8*)(hp + s * 4096 + m * 1024 + vo);
  f.w[6] = *(const bf16x8*)(wp + 6 * 1024 + vo);
#pragma unroll
  for (int n = 0; n < 3; ++n) {
    f.w[n] = *(const bf16x8*)(wp + n * 1024 + vo);
    f.w[3 + n] = *(const bf16x8*)(wp + (3 + n) * 1024 + vo);
  }
}
__device__ __forceinline__ void k2_mma1(f32x4 (&acc)[5][4], const K2Frag& f, int n, int s, int m) {
  if (n < 3) acc[n][m] = mfma(f.w[3 * s + n], f.h[s][m], acc[n][m]);
  else acc[3 + s][m] = mfma(f.w[6], f.h[s][m], acc[3 + s][m]);
}
__device__ __forceinline__ void k2_mma(f32x4 (&acc)[5][4], const K2Frag& f) {
#pragma unroll
  for (int i = 0; i < 4; ++i)
#pragma unroll
    for (int s = 0; s < 2; ++s)
#pragma unroll
      for (int m = 0; m < 4; ++m) k2_mma1(acc, f, (i + 3) & 3, s, m);
}
__device__ __forceinline__ void k2_mma_last(f32x4 (&acc)[5][4], const K2Frag& f, unsigned a0, unsigned a1, bool low) {
  auto put = [&](int n, int m) {
    const f32x4 v = n < 3 ? acc[n][m] : (low ? acc[3][m] : acc[4][m]);
    *(LDS3 f32x4*)(size_t)(((n & 1) ? a1 : a0) + m * 4096 + (n >> 1) * 128) = v;
  };
#pragma unroll
  for (int i = 0; i < 4; ++i)
#pragma unroll
    for (int s = 0; s < 2; ++s)
#pragma unroll
      for (int m = 0; m < 4; ++m) {
        k2_mma1(acc, f, (i + 3) & 3, s, m);
        if (K2_ILV && i > 0 && (m & 1)) {
          SCHED0;
          put((i + 2) & 3, 2 * s + (m >> 1));
          SCHED0;
        }
      }
  if (!K2_ILV) {
    SCHED0;
#pragma unroll
    for (int n = 0; n < 4; ++n)
#pragma unroll
      for (int m = 0; m < 4; ++m) if (n != 2) put(n, m);
  }
#pragma unroll
  for (int m = 0; m < 4; ++m) put(2, m);
}
__device__ __forceinline__ float next_lane(float v) {
  return __builtin_bit_cast(float, __builtin_amdgcn_update_dpp(0, __builtin_bit_cast(int, v), 0x101, 0xf, 0xf, true));
}
template <int PP>
__device__ __forceinline__ void k2_main(f32x4 (&acc)[5][4], const char* hp, const char* wp, unsigned vo, unsigned a0,
                                        unsigned a1, bool low) {
  K2Frag f[2];
  SCHED0;
  k2_load(f[0], hp, wp, vo);
  SCHED0;
  k2_load(f[1], hp + 8192, wp + 7168, vo);
#pragma unroll
  for (int p = 0; p < PP - 1; ++p) {
    SCHED0;
    k2_mma(acc, f[p & 1]);
    SCHED0;
    if (p + 2 < PP) k2_load(f[p & 1], hp + (p + 2) * 8192, wp + (p + 2) * 7168, vo);
  }
  SCHED0;
  k2_mma_last(acc, f[(PP - 1) & 1], a0, a1, low);
  SCHED0;
}

__global__ __launch_bounds__(K2_NW * 64) void k2(const char* __restrict__ ws, u16* __restrict__ out, u64* stp) {
  extern __shared__ __attribute__((aligned(16))) char lds[];
  const int lane = threadIdx.x & 63;
  const int w = __builtin_amdgcn_readfirstlane(threadIdx.x >> 6);
  const int wv = w;
  RSTAMP(10);
  STAMP(11);
  const int xcd = blockIdx.x & 7, idx = blockIdx.x >> 3;
  const int tb = 4 * xcd + (idx >> 3), c = idx & 7;
  const int p0 = (int)((K2_FIRST >> (8 * w)) & 0xff);
  const char* hp = ws + (size_t)(tb * NK2 + 2 * p0) * 4096;
  const char* wp = ws + WB_OFF + (size_t)(c * 42 + p0) * 7168;
  const unsigned vo = lane * 16;
  const int g = lane >> 4, i16 = lane & 15;
  const unsigned lds0 = (unsigned)(size_t)(LDS3 char*)lds;

  f32x4 acc[5][4];
#pragma unroll
  for (int n = 0; n < 5; ++n)
#pragma unroll
    for (int m = 0; m < 4; ++m) acc[n][m] = f32x4{0, 0, 0, 0};
  const float gA = ((const float*)(ws + G_OFF))[64 * tb + (threadIdx.x >> 3)];
  const int cnt = (int)((K2_COUNT >> (4 * w)) & 0xf);
  const unsigned row = lds0 + w * K2_TILE + i16 * 256, x0 = (g ^ (i16 & 7)) << 4;
  const unsigned a0 = row + x0, a1 = row + (x0 ^ 64);
  if (cnt == K2_CA) k2_main<K2_CA>(acc, hp, wp, vo, a0, a1, g < 2);
  else if (cnt == K2_CB) k2_main<K2_CB>(acc, hp, wp, vo, a0, a1, g < 2);
  else k2_main<K2_CC>(acc, hp, wp, vo, a0, a1, g < 2);
  STAMP(12);
  STAMP(15);
  __syncthreads();
  STAMP(7);
  {
    const int t = threadIdx.x >> 3, j = threadIdx.x & 7;
    const unsigned trow = lds0 + t * 256, y0 = ((2 * j) ^ (t & 7)) << 4;
    const unsigned b0 = trow + y0, b1 = trow + (y0 ^ 16);
    f32x4 v0[K2_NW], v1[K2_NW];
#pragma unroll
    for (int ww = 0; ww < K2_NW; ++ww) {
      v0[ww] = *(LDS3 f32x4*)(size_t)(b0 + ww * K2_TILE);
      v1[ww] = *(LDS3 f32x4*)(size_t)(b1 + ww * K2_TILE);
    }
    const float gB = 1.0f - gA;
    f32x4 a0v = {0, 0, 0, 0}, a1v = a0v, b0v = a0v, b1v = a0v;
    bool fa = true, fb = true;
#pragma unroll
    for (int ww = 0; ww < K2_NW; ++ww) {
      if (((K2_FIRST >> (8 * ww)) & 0xff) < 14) {
        a0v = fa ? v0[ww] : a0v + v0[ww];
        a1v = fa ? v1[ww] : a1v + v1[ww];
        fa = false;
      } else {
        b0v = fb ? v0[ww] : b0v + v0[ww];
        b1v = fb ? v1[ww] : b1v + v1[ww];
        fb = false;
      }
    }
    f32x4 s0 = a0v * gA + b0v * gB;
    f32x4 s1 = a1v * gA + b1v * gB;
#pragma unroll
    for (int r = 0; r < 4; ++r) {
      const float e0 = s0[r], e1 = s1[r];
      const float n0 = next_lane(e0), n1 = next_lane(e1);
      s0[r] = j == 6 ? e0 + n0 : e0;
      s1[r] = j == 6 ? e1 + n1 : e1;
    }
    u32x4 v = {pk(s0[0], s0[1]), pk(s0[2], s0[3]), pk(s1[0], s1[1]), pk(s1[2], s1[3])};
    if (j < 7) st16o(out + (size_t)(64 * tb + t) * D + 56 * c + 8 * j, v);
  }
  STAMP(13);
  RSTAMP(14);
}

}

extern "C" void kernel_launch(void* const* d_in, const int* in_sizes, int n_in, void* d_out, int out_size,
                              void* d_ws, size_t ws_size, hipStream_t stream) {
  (void)in_sizes; (void)n_in; (void)out_size; (void)ws_size;
  (void)hipFuncSetAttribute((const void*)k1, hipFuncAttributeMaxDynamicSharedMemorySize, K1_LDS);
  (void)hipFuncSetAttribute((const void*)k2, hipFuncAttributeMaxDynamicSharedMemorySize, K2_LDS);
  k1<<<256, 512, K1_LDS, stream>>>((const char*)d_in[0], (const char*)d_in[1], (const char*)d_in[2],
                                   (const char*)d_in[3], (const char*)d_in[4], (const char*)d_in[6],
                                   (const char*)d_in[7], (const char*)d_in[8], (const char*)d_in[9], (char*)d_ws);
  k2<<<256, K2_NW * 64, K2_LDS, stream>>>((const char*)d_ws, (u16*)d_out, (u64*)((char*)d_ws + ST_OFF));
}
```

```cpp
#include <hip/hip_runtime.h>
#include <stdint.h>

namespace {

typedef unsigned short u16;
typedef unsigned long long u64;
typedef __attribute__((ext_vector_type(8))) __bf16 bf16x8;
typedef __attribute__((ext_vector_type(4))) __bf16 bf16x4;
typedef __attribute__((ext_vector_type(2))) __bf16 bf16x2;
typedef __attribute__((ext_vector_type(4))) float f32x4;
typedef __attribute__((ext_vector_type(2))) float f32x2;
typedef __attribute__((ext_vector_type(4))) unsigned u32x4;
typedef __attribute__((ext_vector_type(2))) unsigned u32x2;
#define LDS3 __attribute__((address_space(3)))

constexpr int S = 2048, D = 448, F = 1792;
constexpr int NK2 = 84;
constexpr size_t HB_BYTES = (size_t)32 * NK2 * 4096;
constexpr size_t WB_OFF = HB_BYTES;
constexpr size_t WB_BYTES = (size_t)8 * 42 * 7 * 1024;
constexpr size_t G_OFF = WB_OFF + WB_BYTES;

__device__ __forceinline__ int pq_base(int xcd) { return 2 * xcd; }
__device__ __forceinline__ int pq_kblock(int s, int tb, int ds) {
  const int r = (ds + tb) & 3;
  const int k = s < 16 ? (s & ~3) + ((s + r) & 3) : 16 + ((s + 4 * r) & 15);
  return (pq_base(tb >> 2) + k) & 31;
}
constexpr int PQ_NS = 5;
constexpr int H_XSLOT = 32768;
constexpr int H_WOFF = 3 * H_XSLOT;
constexpr int H_WSLOT = 16384;
constexpr int K1_LDS = H_WOFF + 4 * H_WSLOT;

template <int IMM>
__device__ __forceinline__ void glds(unsigned voff, const char* sbase, unsigned ldsbase) {
  asm volatile("s_add_u32 m0, %2, %3\n\ts_nop 0\n\tglobal_load_lds_dwordx4 %0, %1"
               :: "v"(voff), "s"(sbase), "s"(ldsbase), "i"(IMM) : "memory", "scc");
}
__device__ __forceinline__ void st16(void* p, u32x4 v) {
  asm volatile("global_store_dwordx4 %0, %1, off sc1\n\ts_nop 1" :: "v"(p), "v"(v) : "memory");
}
__device__ __forceinline__ void st16o(void* p, u32x4 v) {
  asm volatile("global_store_dwordx4 %0, %1, off\n\ts_nop 1" :: "v"(p), "v"(v) : "memory");
}
__device__ __forceinline__ void st4(void* p, float v) {
  asm volatile("global_store_dword %0, %1, off sc1" :: "v"(p), "v"(v) : "memory");
}
template <int N> __device__ __forceinline__ void wait_vm_bar() {
  asm volatile("s_waitcnt vmcnt(%0)\n\ts_barrier" :: "i"(N) : "memory");
}
__device__ __forceinline__ void wait_lgkm_bar() { asm volatile("s_waitcnt lgkmcnt(0)\n\ts_barrier" ::: "memory"); }
__device__ __forceinline__ void bar() { asm volatile("s_barrier" ::: "memory"); }
#define SCHED0 __builtin_amdgcn_sched_barrier(0)

__device__ __forceinline__ bf16x8 lds_b128(unsigned addr) { return *(LDS3 bf16x8*)(addr); }
template <int HI>
__device__ __forceinline__ bf16x8 lds_tr2(unsigned addr) {
  bf16x4 lo = __builtin_amdgcn_ds_read_tr16_b64_v4bf16((LDS3 bf16x4*)(addr));
  bf16x4 hi = __builtin_amdgcn_ds_read_tr16_b64_v4bf16((LDS3 bf16x4*)(addr + HI));
  return __builtin_shufflevector(lo, hi, 0, 1, 2, 3, 4, 5, 6, 7);
}
__device__ __forceinline__ f32x4 mfma(bf16x8 a, bf16x8 b, f32x4 c) {
  return __builtin_amdgcn_mfma_f32_16x16x32_bf16(a, b, c, 0, 0, 0);
}
__device__ __forceinline__ unsigned pk(float a, float b) {
  f32x2 v = {a, b};
  bf16x2 p = __builtin_convertvector(v, bf16x2);
  return __builtin_bit_cast(unsigned, p);
}
__device__ __forceinline__ float sigmoidf_(float z) {
  return __builtin_amdgcn_rcpf(1.0f + __builtin_amdgcn_exp2f(-1.4426950408889634f * z));
}
__device__ __forceinline__ int perm32(int p) { return 8 * ((p & 15) >> 2) + (p & 3) + 4 * (p >> 4); }

__device__ __forceinline__ float dot8(u32x4 a, u32x4 b) {
  float s = 0.f;
#pragma unroll
  for (int j = 0; j < 4; ++j) {
    s = __builtin_fmaf(__builtin_bit_cast(float, a[j] << 16), __builtin_bit_cast(float, b[j] << 16), s);
    s = __builtin_fmaf(__builtin_bit_cast(float, a[j] & 0xffff0000u), __builtin_bit_cast(float, b[j] & 0xffff0000u), s);
  }
  return s;
}
__device__ __forceinline__ void gate_rows(const char* x, const char* gw, char* ws, int row0, int lane) {
  const int row = row0 + (lane >> 2), part = lane & 3;
  u32x4 xv[14], gv[14];
#pragma unroll
  for (int i = 0; i < 14; ++i) {
    xv[i] = *(const u32x4*)(x + row * 896 + (4 * i + part) * 16);
    gv[i] = *(const u32x4*)(gw + (4 * i + part) * 16);
  }
  float z = 0.f;
#pragma unroll
  for (int i = 0; i < 14; ++i) z += dot8(xv[i], gv[i]);
  z += __shfl_xor(z, 1);
  z += __shfl_xor(z, 2);
  if (part == 0) st4(ws + G_OFF + row * 4, sigmoidf_(z));
}

template <int NJ> struct Pq {
  static constexpr int ROWB = NJ * 32;
  static constexpr int XSLOT = 64 * ROWB;
  static constexpr int XOFF = PQ_NS * 16384;
  static constexpr int NX = NJ / 2;
  static constexpr int PER = 4 + NX;
  static constexpr int NH = NJ / 2;
  __device__ static __forceinline__ int swz(int pos) { return NJ == 8 ? (pos & 7) : ((pos >> 1) & 3); }
};

template <int NJ>
__device__ __forceinline__ void pq_loader(const char* cr, const char* ci, const char* x, const char* gw, char* ws,
                                          unsigned lds0, int tb, int ds, int lw, int lane) {
  using P = Pq<NJ>;
  unsigned vC[4], vX[P::NX];
#pragma unroll
  for (int qq = 0; qq < 4; ++qq) {
    int row = 64 * tb + 32 * (lw & 1) + 8 * qq + (lane >> 3);
    int c = (lane & 7) ^ ((4 * (qq & 1) + (lane >> 4)) & 7);
    vC[qq] = row * 4096 + c * 16;
  }
#pragma unroll
  for (int qq = 0; qq < P::NX; ++qq) {
    constexpr int LPR = P::ROWB / 16;
    int pos = (P::NX * lw + qq) * (64 / LPR) + lane / LPR;
    int c = lane % LPR;
    int s = (pos & 32) + perm32(pos & 31);
    vX[qq] = s * 896 + ds * 256 + (((c >> 1) ^ P::swz(pos)) << 5) + ((c & 1) << 4);
  }
  const char* cb0 = (lw >> 1) ? ci : cr;
  const unsigned ldsC = lds0 + lw * 4096, ldsX = lds0 + P::XOFF + P::NX * lw * 1024;
  int st = 0;
  asm volatile("s_nop 4" ::: "memory");
  auto issue = [&](unsigned slot) {
    const int kk = pq_kblock(st, tb, ds);
    const char* cb = cb0 + kk * 128;
    const char* xb = x + kk * (64 * 896);
    const unsigned lc = ldsC + slot * 16384, lx = ldsX + slot * P::XSLOT;
    glds<0>(vC[0], cb, lc);
    glds<1024>(vC[1], cb, lc);
    glds<2048>(vC[2], cb, lc);
    glds<3072>(vC[3], cb, lc);
    glds<0>(vX[0], xb, lx);
    glds<1024>(vX[1], xb, lx);
    if constexpr (P::NX == 4) {
      glds<2048>(vX[2], xb, lx);
      glds<3072>(vX[3], xb, lx);
    }
    ++st;
  };
  issue(0);
  issue(1);
  wait_vm_bar<P::PER>();
  issue(2);
  issue(3);
  issue(4);
  unsigned slot = 0;
  for (int i = 0; i < 32 - PQ_NS; ++i) {
    wait_vm_bar<P::PER * (PQ_NS - 2)>();
    issue(slot);
    slot = (slot == PQ_NS - 1) ? 0 : slot + 1;
  }
  wait_vm_bar<P::PER * 3>();
  wait_vm_bar<P::PER * 2>();
  wait_vm_bar<P::PER * 1>();
  wait_vm_bar<0>();
  if constexpr (NJ == 4) gate_rows(x, gw, ws, 64 * tb + 16 * lw, lane);
}

template <int NH> struct PqFrag { bf16x8 a[NH]; bf16x8 b[4]; };

template <int NH> struct PqAddr { unsigned c[2][2]; unsigned x[2][NH]; };

template <int NJ, int SL, int KK>
__device__ __forceinline__ void pq_read(PqFrag<NJ / 2>& f, const PqAddr<NJ / 2>& a) {
  using P = Pq<NJ>;
  constexpr int RB = P::ROWB, W = SL / 4;
  constexpr int oc = (SL % 4) * 16384, ox = (SL % 4) * P::XSLOT + KK * 32 * RB;
  f.b[0] = lds_b128(a.c[W][KK] + oc);
  f.a[0] = lds_tr2<16 * RB>(a.x[W][0] + ox);
  f.b[1] = lds_b128(a.c[W][KK] + oc + 8192);
  f.b[2] = lds_b128(a.c[W][KK] + oc + 2048);
  f.b[3] = lds_b128(a.c[W][KK] + oc + 8192 + 2048);
#pragma unroll
  for (int j = 1; j < NJ / 2; ++j) f.a[j] = lds_tr2<16 * RB>(a.x[W][j] + ox);
}
template <int NH>
__device__ __forceinline__ void pq_mma(f32x4 (&acc)[NH][4], const PqFrag<NH>& f) {
#pragma unroll
  for (int j = 0; j < NH; ++j)
#pragma unroll
    for (int m = 0; m < 4; ++m) acc[j][m] = mfma(f.a[j], f.b[m], acc[j][m]);
}
template <int NH>
__device__ __forceinline__ void pq_pattern() {
  constexpr int NR = 4 + 2 * NH, NM = 4 * NH;
  constexpr int DBL = 2;
#pragma unroll
  for (int j = 0; j < DBL; ++j) {
    __builtin_amdgcn_sched_group_barrier(0x100, 2, 0);
    __builtin_amdgcn_sched_group_barrier(0x8, 1, 0);
  }
#pragma unroll
  for (int j = 0; j < NR - 2 * DBL; ++j) {
    __builtin_amdgcn_sched_group_barrier(0x100, 1, 0);
    __builtin_amdgcn_sched_group_barrier(0x8, 1, 0);
  }
  __builtin_amdgcn_sched_group_barrier(0x8, NM - (NR - DBL), 0);
}

template <int NJ, int SL>
__device__ __forceinline__ void pq_step(f32x4 (&acc)[NJ / 2][4], PqFrag<NJ / 2>& f0, PqFrag<NJ / 2>& f1,
                                        const PqAddr<NJ / 2>& a) {
  SCHED0;
  pq_read<NJ, SL, 1>(f1, a);
  pq_mma<NJ / 2>(acc, f0);
  pq_pattern<NJ / 2>();
  SCHED0;
  wait_lgkm_bar();
  SCHED0;
  pq_read<NJ, (SL + 1) % PQ_NS, 0>(f0, a);
  pq_mma<NJ / 2>(acc, f1);
  pq_pattern<NJ / 2>();
}
template <int NJ>
__device__ __forceinline__ void pq_steps5(f32x4 (&acc)[NJ / 2][4], PqFrag<NJ / 2>& f0, PqFrag<NJ / 2>& f1,
                                          const PqAddr<NJ / 2>& a) {
  pq_step<NJ, 0>(acc, f0, f1, a);
  pq_step<NJ, 1>(acc, f0, f1, a);
  pq_step<NJ, 2>(acc, f0, f1, a);
  pq_step<NJ, 3>(acc, f0, f1, a);
  pq_step<NJ, 4>(acc, f0, f1, a);
}

template <int NJ>
__device__ __forceinline__ void pq_consumer(char* hb, unsigned lds0, int tb, int ds, int cw, int lane) {
  using P = Pq<NJ>;
  constexpr int NH = P::NH;
  const int g = lane >> 4, i16 = lane & 15;
  const int wm = cw & 1, wn = cw >> 1;
  const int pos = 4 * g + (i16 >> 2);
  PqAddr<NH> a;
#pragma unroll
  for (int W = 0; W < 2; ++W) {
#pragma unroll
    for (int j = 0; j < NH; ++j) {
      a.x[W][j] = lds0 + P::XOFF + W * 4 * P::XSLOT + pos * P::ROWB + (((NH * wn + j) ^ P::swz(pos)) << 5) + 8 * (lane & 3);
      asm volatile("" : "+v"(a.x[W][j]));
    }
#pragma unroll
    for (int k = 0; k < 2; ++k) {
      a.c[W][k] = (lds0 + W * 65536 + (32 * wm + i16) * 128 + ((g ^ (i16 >> 1)) << 4)) ^ (k * 64);
      asm volatile("" : "+v"(a.c[W][k]));
    }
  }
  f32x4 acc[NH][4];
#pragma unroll
  for (int j = 0; j < NH; ++j)
#pragma unroll
    for (int m = 0; m < 4; ++m) acc[j][m] = f32x4{0, 0, 0, 0};
  PqFrag<NH> f0, f1;
  bar();
  pq_read<NJ, 0, 0>(f0, a);
#pragma unroll 1
  for (int it = 0; it < 6; ++it) pq_steps5<NJ>(acc, f0, f1, a);
  pq_step<NJ, 0>(acc, f0, f1, a);
  SCHED0;
  pq_read<NJ, 1, 1>(f1, a);
  pq_mma<NH>(acc, f0);
  pq_pattern<NH>();
  SCHED0;
  pq_mma<NH>(acc, f1);
  char* dst = hb + ((size_t)(tb * NK2 + 8 * ds + NH * wn) * 4 + 2 * wm) * 1024 + lane * 16;
#pragma unroll
  for (int j = 0; j < NH; ++j)
#pragma unroll
    for (int mt = 0; mt < 2; ++mt) {
      const f32x4 p = acc[j][2 * mt], q = acc[j][2 * mt + 1];
      u32x4 v = {pk(p[0], p[1]), pk(p[2], p[3]), pk(q[0], q[1]), pk(q[2], q[3])};
      st16(dst + j * 4096 + mt * 1024, v);
    }
}

template <int NJ>
__device__ __forceinline__ void pq_task(const char* x, const char* cr, const char* ci, const char* gw, char* ws,
                                        unsigned lds0, int tb, int ds, int w, int lane) {
  if (w < 4) pq_consumer<NJ>(ws, lds0, tb, ds, w, lane);
  else pq_loader<NJ>(cr, ci, x, gw, ws, lds0, tb, ds, w - 4, lane);
}

struct HAddr { unsigned vW[2]; unsigned vX[4]; const char* w; const char* x; unsigned lW; unsigned lX; };


template <int U, int Q>
__device__ __forceinline__ void h_issue_w(const HAddr& a) {
  glds<(U & 3) * H_WSLOT + Q * 1024>(a.vW[Q], a.w + (size_t)U * (32 * 3584), a.lW);
}
template <int J, int Q>
__device__ __forceinline__ void h_issue_x(const HAddr& a) {
  glds<(J % 3) * H_XSLOT + Q * 1024>(a.vX[Q], a.x + J * 128, a.lX);
}
template <int U, int I>
__device__ __forceinline__ void h_issue(const HAddr& a) {
  if constexpr (I < 2) {
    if constexpr (U + 3 <= 13) h_issue_w<U + 3, I>(a);
  } else {
    if constexpr (U / 2 + 2 <= 6) h_issue_x<U / 2 + 2, 2 * (U & 1) + I - 2>(a);
  }
}
constexpr int h_window(int U) { return U < 0 ? 6 : (U + 3 <= 13 ? 2 : 0) + (U / 2 + 2 <= 6 ? 2 : 0); }

template <int U, int N>
__device__ __forceinline__ bf16x8 h_read_a(const unsigned (&vw)[4]) {
  return lds_tr2<4096>(vw[N >> 1] + (U & 3) * H_WSLOT + (N & 1) * 8192);
}
template <int U>
__device__ __forceinline__ void h_read_b(bf16x8 (&b)[4], unsigned vx) {
  constexpr int J = U >> 1, KK = U & 1;
  const unsigned base = (vx ^ (KK * 64)) + (J % 3 == 2 ? 2 * H_XSLOT : 0);
#pragma unroll
  for (int m = 0; m < 4; ++m) b[m] = lds_b128(base + (J % 3 == 1 ? H_XSLOT : 0) + m * 2048);
}

template <int Q, int MT>
__device__ __forceinline__ void h_store(const f32x4 (&acc)[8][4], char* dst) {
  unsigned hp[4];
#pragma unroll
  for (int e = 0; e < 2; ++e) {
    const f32x4 ga = acc[4 * Q + 2 * e][MT], up = acc[4 * Q + 2 * e + 1][MT];
    f32x4 h = ga * -1.4426950408889634f;
#pragma unroll
    for (int r = 0; r < 4; ++r) h[r] = __builtin_amdgcn_exp2f(h[r]);
    h += 1.0f;
#pragma unroll
    for (int r = 0; r < 4; ++r) h[r] = __builtin_amdgcn_rcpf(h[r]);
    h *= up;
    hp[2 * e] = pk(h[0], h[1]);
    hp[2 * e + 1] = pk(h[2], h[3]);
  }
  const u32x4 v = {hp[0], hp[1], hp[2], hp[3]};
  st16(dst + Q * 4096 + MT * 1024, v);
}

template <int G, int U, int N>
__device__ __forceinline__ void h_frag(f32x4 (&acc)[8][4], bf16x8 (&a)[4], bf16x8 (&bc)[4], bf16x8 (&bn)[4],
                                       const HAddr& ad, const unsigned (&vw)[4], unsigned vx, char* dst) {
  if constexpr (N == 4 && U < 13) {
    SCHED0;
    wait_vm_bar<h_window(U - 1)>();
    SCHED0;
    h_read_b<U + 1>(bn, vx);
  }
  const bf16x8 cur = a[N & 3];
#pragma unroll
  for (int m = 0; m < 4; ++m) acc[N][m] = mfma(cur, bc[m], acc[N][m]);
  if constexpr (N + 4 < 8) a[N & 3] = h_read_a<U, N + 4>(vw);
  else if constexpr (U < 13) a[N & 3] = h_read_a<U + 1, N - 4>(vw);
  if constexpr ((N & 1) == G) {
    constexpr int slot = ((N + 4) & 7) >> 1;
    constexpr int win = (N >= 4) ? U : U - 1;
    if constexpr (win >= 0 && win < 13) h_issue<win, slot>(ad);
  }
  if constexpr (U == 13 && N >= 4) h_store<0, N - 4>(acc, dst);
  SCHED0;
}
template <int G, int U, int N>
__device__ __forceinline__ void h_frags(f32x4 (&acc)[8][4], bf16x8 (&a)[4], bf16x8 (&bc)[4], bf16x8 (&bn)[4],
                                        const HAddr& ad, const unsigned (&vw)[4], unsigned vx, char* dst) {
  h_frag<G, U, N>(acc, a, bc, bn, ad, vw, vx, dst);
  if constexpr (N < 7) h_frags<G, U, N + 1>(acc, a, bc, bn, ad, vw, vx, dst);
}
template <int G, int U>
__device__ __forceinline__ void h_steps(f32x4 (&acc)[8][4], bf16x8 (&a)[4], bf16x8 (&b0)[4], bf16x8 (&b1)[4],
                                        const HAddr& ad, const unsigned (&vw)[4], unsigned vx, char* dst) {
  if constexpr ((U & 1) == 0) h_frags<G, U, 0>(acc, a, b0, b1, ad, vw, vx, dst);
  else h_frags<G, U, 0>(acc, a, b1, b0, ad, vw, vx, dst);
  if constexpr (U < 13) h_steps<G, U + 1>(acc, a, b0, b1, ad, vw, vx, dst);
}

__device__ __forceinline__ void h_task(const char* x, const char* wg, const char* wu, char* ws, unsigned lds0, int fb,
                                       int rb, int w, int lane) {
  const int g = lane >> 4, i16 = lane & 15;
  const int wt = w >> 1, wn = w & 1;
  HAddr ad;
#pragma unroll
  for (int qq = 0; qq < 2; ++qq) {
    int pos = 4 * ((2 * w + qq) & 7) + (lane >> 4);
    int c = lane & 15;
    ad.vW[qq] = perm32(pos) * 3584 + fb * 256 + (((c >> 1) ^ (pos & 7)) << 5) + ((c & 1) << 4);
  }
  ad.w = (w < 4) ? wg : wu;
  ad.lW = lds0 + H_WOFF + w * 2048;
#pragma unroll
  for (int qq = 0; qq < 4; ++qq) {
    int row = 256 * rb + 32 * w + 8 * qq + (lane >> 3);
    int c = (lane & 7) ^ ((4 * (qq & 1) + (lane >> 4)) & 7);
    ad.vX[qq] = row * 896 + c * 16;
  }
  ad.x = x;
  ad.lX = lds0 + w * 4096;
  const int pos = 4 * g + (i16 >> 2);
  unsigned vw[4];
#pragma unroll
  for (int j = 0; j < 4; ++j) vw[j] = lds0 + H_WOFF + pos * 256 + (((4 * wn + j) ^ (pos & 7)) << 5) + 8 * (lane & 3);
  const unsigned vx = lds0 + (64 * wt + i16) * 128 + ((g ^ (i16 >> 1)) << 4);
  char* dst = ws + ((size_t)((4 * rb + wt) * NK2 + 28 + 4 * fb + 2 * wn) * 4) * 1024 + lane * 16;
  asm volatile("s_nop 4" ::: "memory");
  h_issue_w<0, 0>(ad); h_issue_w<0, 1>(ad);
  h_issue_x<0, 0>(ad); h_issue_x<0, 1>(ad); h_issue_x<0, 2>(ad); h_issue_x<0, 3>(ad);
  h_issue_w<1, 0>(ad); h_issue_w<1, 1>(ad);

  f32x4 acc[8][4];
#pragma unroll
  for (int n = 0; n < 8; ++n)
#pragma unroll
    for (int m = 0; m < 4; ++m) acc[n][m] = f32x4{0, 0, 0, 0};
  bf16x8 a[4], b0[4], b1[4];
  wait_vm_bar<2>();
  h_issue_w<2, 0>(ad); h_issue_w<2, 1>(ad);
  h_issue_x<1, 0>(ad); h_issue_x<1, 1>(ad); h_issue_x<1, 2>(ad); h_issue_x<1, 3>(ad);
  SCHED0;
  h_read_b<0>(b0, vx);
  a[0] = h_read_a<0, 0>(vw); a[1] = h_read_a<0, 1>(vw); a[2] = h_read_a<0, 2>(vw); a[3] = h_read_a<0, 3>(vw);
  if (w < 4) h_steps<0, 0>(acc, a, b0, b1, ad, vw, vx, dst);
  else h_steps<1, 0>(acc, a, b0, b1, ad, vw, vx, dst);
  SCHED0;

  h_store<1, 0>(acc, dst); h_store<1, 1>(acc, dst); h_store<1, 2>(acc, dst); h_store<1, 3>(acc, dst);
}

constexpr int SD_AOFF = 448 * 224;
constexpr int SD_ASZ = 25 * 1024;

template <int I>
__device__ __forceinline__ void side_issue_b(const char* wd, unsigned lds0, int j, int w, int lane) {
  const int t = w + 8 * I;
  if (I < 12 || t < 98) {
    const unsigned q = 64 * t + lane, row = q / 14, ch = q - 14 * row;
    glds<I * 8192>((448 * (j >> 2) + row) * 896 + 224 * (j & 3) + 16 * ch, wd, lds0 + w * 1024);
  }
  if constexpr (I < 12) side_issue_b<I + 1>(wd, lds0, j, w, lane);
}
template <int I>
__device__ __forceinline__ void side_issue_a(const char* wre, const char* wim, unsigned lds0, int j, int w, int lane) {
  const int u = w + 8 * I;
  if (I < 6 || u < 50) {
    const int m = u >= 25, t = u - 25 * m;
    const unsigned q0 = 64 * t + lane, q = q0 < 1568 ? q0 : 1567, row = q / 28, ch = q - 28 * row;
    glds<SD_AOFF>((56 * (j >> 1) + row) * 896 + 448 * (j & 1) + 16 * ch, m ? wim : wre, lds0 + m * SD_ASZ + t * 1024);
  }
  if constexpr (I < 6) side_issue_a<I + 1>(wre, wim, lds0, j, w, lane);
}

__device__ __forceinline__ void side_touch(unsigned& sink, const char* cm, int xcd, int w, int lane, int n) {
  const unsigned row = 256 * xcd + 32 * w + 16 * (n & 1) + (lane >> 2);
  const unsigned vo = row * 4096 + ((pq_base(xcd) + 16 + 4 * (lane & 3) + (n >> 1)) & 31) * 128;
  asm volatile("global_load_dword %0, %1, %2 sc1" : "+v"(sink) : "v"(vo), "s"(cm) : "memory");
}
constexpr int SD_NTOUCH = 6;

__device__ __forceinline__ void side_task(const char* wre, const char* wim, const char* wd, const char* cr, const char* ci,
                                          char* ws, unsigned lds0, int j, int w, int lane) {
  char* wb = ws + WB_OFF + lane * 16;
  const int g = lane >> 4, i16 = lane & 15;
  unsigned sink = 0;
  const char* cm = (j & 1) ? ci : cr;
  asm volatile("s_nop 4" ::: "memory");
  side_issue_a<0>(wre, wim, lds0, j, w, lane);
  side_issue_b<0>(wd, lds0, j, w, lane);
  wait_vm_bar<0>();
#pragma unroll
  for (int n = 0; n < SD_NTOUCH; ++n) side_touch(sink, cm, j >> 1, w, lane, n);
  u32x2 lo[7], hi[7];
#pragma unroll
  for (int i = 0; i < 7; ++i) {
    const int n = (i < 6 || w == 0) ? w + 8 * i : 0, pp = n / 7, sl = n - 7 * pp;
    const int h = sl < 6 ? sl / 3 : (i16 >> 3);
    const int e = sl < 6 ? 16 * (sl % 3) + i16 : 48 + (i16 & 7);
    const unsigned ad = lds0 + SD_AOFF + e * 448 + pp * 64 + 32 * h + 8 * g;
    lo[i] = *(LDS3 u32x2*)(ad);
    hi[i] = *(LDS3 u32x2*)(ad + SD_ASZ);
  }
  bf16x8 fb[13];
#pragma unroll
  for (int i = 0; i < 13; ++i) {
    const int n = (i < 12 || w < 2) ? w + 8 * i : 0, c = n / 49, rem = n - 49 * c, pp = rem / 7, sl = rem - 7 * pp;
    const int row = 64 * pp + 32 * (sl < 6 ? sl / 3 : ((lane & 3) >> 1)) + 4 * g + (i16 >> 2);
    const int col = 112 * c + (sl < 6 ? 32 * (sl % 3) + 8 * (lane & 3) : 96 + 8 * (lane & 1));
    fb[i] = lds_tr2<16 * 224>(lds0 + row * 224 + col);
  }
#pragma unroll
  for (int i = 0; i < 7; ++i) {
    const int n = w + 8 * i;
    const u32x4 v = {lo[i][0], lo[i][1], hi[i][0] ^ 0x80008000u, hi[i][1] ^ 0x80008000u};
    if (i < 6 || n < 49) st16(wb + (size_t)(((j >> 1) * 42 + 7 * (j & 1)) * 7 + n) * 1024, v);
  }
#pragma unroll
  for (int i = 0; i < 13; ++i) {
    const int n = w + 8 * i, c = n / 49, rem = n - 49 * c;
    if (i < 12 || n < 98)
      st16(wb + (size_t)(((2 * (j & 3) + c) * 42 + 14 + 7 * (j >> 2)) * 7 + rem) * 1024, __builtin_bit_cast(u32x4, fb[i]));
  }
  asm volatile("s_waitcnt vmcnt(0)" :: "v"(sink) : "memory");
}

__global__ __launch_bounds__(512, 2) void k1(const char* __restrict__ x, const char* __restrict__ cr,
                                             const char* __restrict__ ci, const char* __restrict__ wre,
                                             const char* __restrict__ wim, const char* __restrict__ wg,
                                             const char* __restrict__ wu, const char* __restrict__ wd,
                                             const char* __restrict__ gw, char* __restrict__ ws) {
  extern __shared__ __attribute__((aligned(16))) char lds[];
  const unsigned lds0 = (unsigned)(size_t)(LDS3 char*)lds;
  const int lane = threadIdx.x & 63;
  const int w = __builtin_amdgcn_readfirstlane(threadIdx.x >> 6);
  const int xcd = blockIdx.x & 7, idx = blockIdx.x >> 3;
  if (idx < 12) {
    const int tl = idx / 3;
    pq_task<8>(x, cr, ci, gw, ws, lds0, 4 * xcd + tl, idx - 3 * tl, w, lane);
  } else if (idx < 16) {
    pq_task<4>(x, cr, ci, gw, ws, lds0, 4 * xcd + idx - 12, 3, w, lane);
  } else if (idx < 30) {
    const int id = 14 * xcd + idx - 16;
    h_task(x, wg, wu, ws, lds0, id >> 3, id & 7, w, lane);
  } else {
    side_task(wre, wim, wd, cr, ci, ws, lds0, 2 * xcd + idx - 30, w, lane);
  }
}

constexpr int K2_NW = 8;
constexpr u64 K2_FIRST = 0x25090400201a140eull;
constexpr unsigned K2_COUNT = 0x55545666u;
constexpr int K2_TILE = 64 * 256;
constexpr int K2_LDS = K2_NW * K2_TILE;

struct K2Frag { bf16x8 h[2][4]; bf16x8 w[7]; };

__device__ __forceinline__ void k2_load(K2Frag& f, const char* hp, const char* wp, unsigned vo) {
#pragma unroll
  for (int s = 0; s < 2; ++s)
#pragma unroll
    for (int m = 0; m < 4; ++m) f.h[s][m] = *(const bf16x8*)(hp + s * 4096 + m * 1024 + vo);
  f.w[6] = *(const bf16x8*)(wp + 6 * 1024 + vo);
#pragma unroll
  for (int n = 0; n < 3; ++n) {
    f.w[n] = *(const bf16x8*)(wp + n * 1024 + vo);
    f.w[3 + n] = *(const bf16x8*)(wp + (3 + n) * 1024 + vo);
  }
}
__device__ __forceinline__ void k2_mma1(f32x4 (&acc)[5][4], const K2Frag& f, int n, int s, int m) {
  if (n < 3) acc[n][m] = mfma(f.w[3 * s + n], f.h[s][m], acc[n][m]);
  else acc[3 + s][m] = mfma(f.w[6], f.h[s][m], acc[3 + s][m]);
}
__device__ __forceinline__ void k2_mma(f32x4 (&acc)[5][4], const K2Frag& f) {
#pragma unroll
  for (int i = 0; i < 4; ++i)
#pragma unroll
    for (int s = 0; s < 2; ++s)
#pragma unroll
      for (int m = 0; m < 4; ++m) k2_mma1(acc, f, (i + 3) & 3, s, m);
}
__device__ __forceinline__ void k2_mma_last(f32x4 (&acc)[5][4], const K2Frag& f, unsigned a0, unsigned a1, bool low) {
  auto put = [&](int n, int m) {
    const f32x4 v = n < 3 ? acc[n][m] : (low ? acc[3][m] : acc[4][m]);
    *(LDS3 f32x4*)(size_t)(((n & 1) ? a1 : a0) + m * 4096 + (n >> 1) * 128) = v;
  };
#pragma unroll
  for (int i = 0; i < 4; ++i)
#pragma unroll
    for (int s = 0; s < 2; ++s)
#pragma unroll
      for (int m = 0; m < 4; ++m) {
        k2_mma1(acc, f, (i + 3) & 3, s, m);
        if (i > 0 && (m & 1)) {
          SCHED0;
          put((i + 2) & 3, 2 * s + (m >> 1));
          SCHED0;
        }
      }
#pragma unroll
  for (int m = 0; m < 4; ++m) put(2, m);
}
__device__ __forceinline__ float next_lane(float v) {
  return __builtin_bit_cast(float, __builtin_amdgcn_update_dpp(0, __builtin_bit_cast(int, v), 0x101, 0xf, 0xf, true));
}
template <int PP>
__device__ __forceinline__ void k2_main(f32x4 (&acc)[5][4], const char* hp, const char* wp, unsigned vo, unsigned a0,
                                        unsigned a1, bool low) {
  K2Frag f[2];
  SCHED0;
  k2_load(f[0], hp, wp, vo);
  SCHED0;
  k2_load(f[1], hp + 8192, wp + 7168, vo);
#pragma unroll
  for (int p = 0; p < PP - 1; ++p) {
    SCHED0;
    k2_mma(acc, f[p & 1]);
    SCHED0;
    if (p + 2 < PP) k2_load(f[p & 1], hp + (p + 2) * 8192, wp + (p + 2) * 7168, vo);
  }
  SCHED0;
  k2_mma_last(acc, f[(PP - 1) & 1], a0, a1, low);
  SCHED0;
}

__global__ __launch_bounds__(K2_NW * 64) void k2(const char* __restrict__ ws, u16* __restrict__ out) {
  extern __shared__ __attribute__((aligned(16))) char lds[];
  const int lane = threadIdx.x & 63;
  const int w = __builtin_amdgcn_readfirstlane(threadIdx.x >> 6);
  const int xcd = blockIdx.x & 7, idx = blockIdx.x >> 3;
  const int tb = 4 * xcd + (idx >> 3), c = idx & 7;
  const int p0 = (int)((K2_FIRST >> (8 * w)) & 0xff);
  const char* hp = ws + (size_t)(tb * NK2 + 2 * p0) * 4096;
  const char* wp = ws + WB_OFF + (size_t)(c * 42 + p0) * 7168;
  const unsigned vo = lane * 16;
  const int g = lane >> 4, i16 = lane & 15;
  const unsigned lds0 = (unsigned)(size_t)(LDS3 char*)lds;

  f32x4 acc[5][4];
#pragma unroll
  for (int n = 0; n < 5; ++n)
#pragma unroll
    for (int m = 0; m < 4; ++m) acc[n][m] = f32x4{0, 0, 0, 0};
  const float gA = ((const float*)(ws + G_OFF))[64 * tb + (threadIdx.x >> 3)];
  const int cnt = (int)((K2_COUNT >> (4 * w)) & 0xf);
  const unsigned row = lds0 + w * K2_TILE + i16 * 256, x0 = (g ^ (i16 & 7)) << 4;
  const unsigned a0 = row + x0, a1 = row + (x0 ^ 64);
  if (cnt == 6) k2_main<6>(acc, hp, wp, vo, a0, a1, g < 2);
  else if (cnt == 5) k2_main<5>(acc, hp, wp, vo, a0, a1, g < 2);
  else k2_main<4>(acc, hp, wp, vo, a0, a1, g < 2);
  __syncthreads();
  {
    const int t = threadIdx.x >> 3, j = threadIdx.x & 7;
    const unsigned trow = lds0 + t * 256, y0 = ((2 * j) ^ (t & 7)) << 4;
    const unsigned b0 = trow + y0, b1 = trow + (y0 ^ 16);
    f32x4 v0[K2_NW], v1[K2_NW];
#pragma unroll
    for (int ww = 0; ww < K2_NW; ++ww) {
      v0[ww] = *(LDS3 f32x4*)(size_t)(b0 + ww * K2_TILE);
      v1[ww] = *(LDS3 f32x4*)(size_t)(b1 + ww * K2_TILE);
    }
    const float gB = 1.0f - gA;
    f32x4 a0v = {0, 0, 0, 0}, a1v = a0v, b0v = a0v, b1v = a0v;
    bool fa = true, fb = true;
#pragma unroll
    for (int ww = 0; ww < K2_NW; ++ww) {
      if (((K2_FIRST >> (8 * ww)) & 0xff) < 14) {
        a0v = fa ? v0[ww] : a0v + v0[ww];
        a1v = fa ? v1[ww] : a1v + v1[ww];
        fa = false;
      } else {
        b0v = fb ? v0[ww] : b0v + v0[ww];
        b1v = fb ? v1[ww] : b1v + v1[ww];
        fb = false;
      }
    }
    f32x4 s0 = a0v * gA + b0v * gB;
    f32x4 s1 = a1v * gA + b1v * gB;
#pragma unroll
    for (int r = 0; r < 4; ++r) {
      const float e0 = s0[r], e1 = s1[r];
      const float n0 = next_lane(e0), n1 = next_lane(e1);
      s0[r] = j == 6 ? e0 + n0 : e0;
      s1[r] = j == 6 ? e1 + n1 : e1;
    }
    u32x4 v = {pk(s0[0], s0[1]), pk(s0[2], s0[3]), pk(s1[0], s1[1]), pk(s1[2], s1[3])};
    if (j < 7) st16o(out + (size_t)(64 * tb + t) * D + 56 * c + 8 * j, v);
  }
}

}

extern "C" void kernel_launch(void* const* d_in, const int* in_sizes, int n_in, void* d_out, int out_size,
                              void* d_ws, size_t ws_size, hipStream_t stream) {
  (void)in_sizes; (void)n_in; (void)out_size; (void)ws_size;
  (void)hipFuncSetAttribute((const void*)k1, hipFuncAttributeMaxDynamicSharedMemorySize, K1_LDS);
  (void)hipFuncSetAttribute((const void*)k2, hipFuncAttributeMaxDynamicSharedMemorySize, K2_LDS);
  k1<<<256, 512, K1_LDS, stream>>>((const char*)d_in[0], (const char*)d_in[1], (const char*)d_in[2],
                                   (const char*)d_in[3], (const char*)d_in[4], (const char*)d_in[6],
                                   (const char*)d_in[7], (const char*)d_in[8], (const char*)d_in[9], (char*)d_ws);
  k2<<<256, K2_NW * 64, K2_LDS, stream>>>((const char*)d_ws, (u16*)d_out);
}
```
